```python
import torch

def _softmax(x, axis=-1):
    return torch.softmax(x, dim=axis)

def _gelu(x):
    return torch.nn.functional.gelu(x, approximate='tanh')

def _silu(x):
    return torch.nn.functional.silu(x)

def _sigmoid(x):
    return torch.sigmoid(x)

def _layernorm(x, axis=-1, eps=1e-05):
    m = x.mean(dim=axis, keepdim=True)
    v = x.var(dim=axis, keepdim=True, unbiased=False)
    return (x - m) / torch.sqrt(v + eps)

def _scatter_add(tgt, idx, src, axis=0):
    return tgt.clone().index_add_(axis, idx, src)

def _segment_sum(src, seg, n):
    out = torch.zeros((n,) + src.shape[1:], dtype=src.dtype)
    return out.index_add_(0, seg, src)

def _pad(x, pw):
    flat = tuple((v for pair in reversed(pw) for v in pair))
    return torch.nn.functional.pad(x, flat)
kernel_name = 'adaLN_cond__ssm_conv_s4__glu_gated_swiglu__prompt'

def setup_inputs():

    _w = lambda a: (lambda t: t.to(torch.bfloat16) if t.is_floating_point() else t)(torch.as_tensor(a))

    def _A_setup():
        B, L, D, N = (1, 2048, 576, 128)
        A_re = -(lambda _x: torch.exp(torch.as_tensor(_x)))((lambda *s: torch.randn(*s, dtype=torch.bfloat16) * 3.2)(D, N))
        A_im = torch.pi * (lambda *s: torch.randn(*s, dtype=torch.bfloat16) * 0.1)(D, N)
        B_re = (lambda *s: torch.randn(*s, dtype=torch.bfloat16) * 0.1)(D, N)
        B_im = (lambda *s: torch.randn(*s, dtype=torch.bfloat16) * 0.1)(D, N)
        C_re = (lambda *s: torch.randn(*s, dtype=torch.bfloat16) * 0.1)(D, N)
        C_im = (lambda *s: torch.randn(*s, dtype=torch.bfloat16) * 0.1)(D, N)
        log_dt = (lambda *s: torch.randn(*s, dtype=torch.bfloat16) * 0.1)(D) * 0.5 - 4.0
        Dskip = (lambda *s: torch.randn(*s, dtype=torch.bfloat16) * 0.1)(D)
        return {'u': _w((lambda *s: torch.randn(*s, dtype=torch.bfloat16) * 0.05)(B, L, D)), 'A_re': _w(A_re), 'A_im': _w(A_im), 'B_re': _w(B_re), 'B_im': _w(B_im), 'C_re': _w(C_re), 'C_im': _w(C_im), 'log_dt': _w(log_dt), 'Dskip': _w(Dskip)}
    _A_in = _A_setup()

    def _B_setup():
        B, S, D, F = (1, 2048, 576, 4608)
        return {'x': _w((lambda *s: torch.randn(*s, dtype=torch.bfloat16) * 0.05)(B, S, D)), 'w1': _w((lambda *s: torch.randn(*s, dtype=torch.bfloat16) * 1.6)(D, F) * (2.0 / D) ** 0.5), 'w3': _w((lambda *s: torch.randn(*s, dtype=torch.bfloat16) * 1.6)(D, F) * (2.0 / D) ** 0.5), 'w2': _w((lambda *s: torch.randn(*s, dtype=torch.bfloat16) * 1.6)(F, D) * (2.0 / F) ** 0.5)}
    _B_in = _B_setup()
    return {'A_u': _A_in['u'], 'A_A_re': _A_in['A_re'], 'A_A_im': _A_in['A_im'], 'A_B_re': _A_in['B_re'], 'A_B_im': _A_in['B_im'], 'A_C_re': _A_in['C_re'], 'A_C_im': _A_in['C_im'], 'A_log_dt': _A_in['log_dt'], 'A_Dskip': _A_in['Dskip'], 'B_x': _B_in['x'], 'B_w1': _B_in['w1'], 'B_w3': _B_in['w3'], 'B_w2': _B_in['w2'], 'c': _w((lambda *s: torch.randn(*s, dtype=torch.bfloat16) * 0.4)(1, 576) * 8.0), 'w_mod': _w((lambda *s: torch.randn(*s, dtype=torch.bfloat16) * 0.4)(576, 6 * 576) * 576 ** (-0.5) * 8.0)}

def reference(A_u, A_A_re, A_A_im, A_B_re, A_B_im, A_C_re, A_C_im, A_log_dt, A_Dskip, B_x, B_w1, B_w3, B_w2, c, w_mod):

    def _A_ref(u, A_re, A_im, B_re, B_im, C_re, C_im, log_dt, Dskip):
        Bsz, L, D = u.shape
        dt = torch.exp(log_dt)[:, None]
        A = A_re + 1j * A_im
        Bc = B_re + 1j * B_im
        Cc = C_re + 1j * C_im
        logdA = dt * A
        dB = dt * Bc
        l = (lambda x, dt: x.to(dt))(torch.arange(L), A_re.dtype)
        P = torch.exp(logdA[:, :, None] * l[None, None, :])
        K = torch.einsum('dn,dnl,dn->dl', Cc, P, dB).real
        L2 = 2 * L
        Kp = _pad(K, ((0, 0), (0, L)))
        up = (lambda x, *d: x.permute(*d))(u, 0, 2, 1)
        up = _pad(up, ((0, 0), (0, 0), (0, L)))
        Kf = (lambda x, axes=(-2, -1): torch.fft.rfft2(x.float(), dim=axes))(Kp[:, None, :], axes=(-2, -1))
        Uf = (lambda x, axes=(-2, -1): torch.fft.rfft2(x.float(), dim=axes))(up[:, :, None, :], axes=(-2, -1))
        Yf = Uf * Kf[None, :, :, :]
        y = (lambda x, s, axes=(-2, -1): torch.fft.irfft2(x, s=s, dim=axes).to(torch.bfloat16))(Yf, (1, L2), axes=(-2, -1))[:, :, 0, :L]
        y = (lambda x, *d: x.permute(*d))(y, 0, 2, 1) + u * Dskip[None, None, :]
        return y

    def _B_ref(x, w1, w3, w2):
        gate = torch.einsum('bsd,df->bsf', x, w1)
        up = torch.einsum('bsd,df->bsf', x, w3)
        h = _silu(gate) * up
        out = torch.einsum('bsf,fd->bsd', h, w2)
        return out
    x = A_u
    mod = (_silu(c) @ w_mod).reshape(c.shape[0], 6, 1, -1)
    sa, ca, ga, sb, cb, gb = (mod[:, 0].clone(), mod[:, 1].clone(), mod[:, 2].clone(), mod[:, 3].clone(), mod[:, 4].clone(), mod[:, 5].clone())
    h = _layernorm(x, axis=-1) * (1 + ca) + sa
    x = x + ga * _A_ref(h, A_A_re, A_A_im, A_B_re, A_B_im, A_C_re, A_C_im, A_log_dt, A_Dskip)
    h = _layernorm(x, axis=-1) * (1 + cb) + sb
    x = x + gb * _B_ref(h, B_w1, B_w3, B_w2)
    return x
```

```cpp
#include <hip/hip_runtime.h>
#include <hip/hip_bf16.h>
#include <stdint.h>

typedef __attribute__((ext_vector_type(8))) __bf16 bf16x8;
typedef __attribute__((ext_vector_type(2))) __bf16 bf16x2;
typedef __attribute__((ext_vector_type(8))) _Float16 f16x8;
typedef __attribute__((ext_vector_type(4))) float f32x4;
typedef __attribute__((ext_vector_type(2))) float f32x2;
typedef __attribute__((ext_vector_type(4))) unsigned u32x4;
typedef __attribute__((ext_vector_type(2))) unsigned u32x2;
typedef __attribute__((ext_vector_type(4))) short s16x4;
typedef unsigned short u16;
#define LDSP __attribute__((address_space(3)))
#define DEV __device__ __forceinline__

constexpr int L = 2048, D = 576, NM = 128, F = 4608;
constexpr int KR_STRIDE = 2064;
constexpr int MODK = 3;
constexpr int FR_S = 1944;

constexpr size_t WS_MODP = 0;
constexpr size_t WS_MODF = 49152;
constexpr size_t WS_XT   = 65536;
constexpr size_t WS_ZT   = WS_XT + (size_t)D * L * 2;
constexpr size_t WS_X1T  = WS_ZT + (size_t)D * L * 2;
constexpr size_t WS_KR   = WS_X1T + (size_t)D * L * 2;
constexpr size_t WS_WT   = (WS_KR + (size_t)D * KR_STRIDE * 2 + 4095) / 4096 * 4096;
constexpr size_t WS_P    = WS_WT + (size_t)8 * FR_S * 1024;

DEV float bf2f(unsigned v) { return __uint_as_float(v << 16); }
DEV float bflo(unsigned v) { return __uint_as_float(v << 16); }
DEV float bfhi(unsigned v) { return __uint_as_float(v & 0xffff0000u); }
DEV float rb(float x) { return (float)(__bf16)x; }
DEV unsigned pk2(float a, float b) { f32x2 v = {a, b}; bf16x2 r = __builtin_convertvector(v, bf16x2); return __builtin_bit_cast(unsigned, r); }
DEV float elem(const u32x4& v, int i) { unsigned w = v[i >> 1]; return (i & 1) ? bfhi(w) : bflo(w); }
DEV float modval(const float* mp, int idx) { return rb(mp[idx] + mp[3456 + idx] + mp[2 * 3456 + idx]); }
DEV int wave_frag_off(int w) { return w * 162 + (w < 4 ? 90 * w : 360 + 72 * (w - 4)); }

DEV void retile_unit(int u, const u16* w1, const u16* w3, const u16* w2, char* wt, char* smem) {
  const int tid = threadIdx.x, lane = tid & 63, wv = tid >> 6;
  const int s = u & 7;
  int v = u >> 3;
  const int g = lane >> 4, qq = (lane & 15) >> 2, pp = lane & 3;
  if (v < 54) {
    const int ks = v / 3, th = v % 3;
    u32x4 r[6];
#pragma unroll
    for (int i = 0; i < 6; ++i) {
      int c = tid + 256 * i; int mat = c / 768; int cc = c % 768; int row = cc / 24, ch = cc % 24;
      const u16* src = (mat ? w3 : w1) + (size_t)(32 * ks + row) * F + 576 * s + 192 * th + ch * 8;
      r[i] = *(const u32x4*)src;
    }
#pragma unroll
    for (int i = 0; i < 6; ++i) *(u32x4*)(smem + (tid + 256 * i) * 16) = r[i];
    __syncthreads();
#pragma unroll
    for (int i = 0; i < 6; ++i) {
      int cj = wv * 6 + i; int cg = 24 * th + cj; int w = cg / 9, j = cg % 9;
      unsigned a = (pp >> 1) * 12288 + (8 * g + qq) * 384 + cj * 16 + (pp & 1) * 8;
      s16x4 lo = __builtin_amdgcn_ds_read_tr16_b64_v4i16((LDSP s16x4*)(smem + a));
      s16x4 hi = __builtin_amdgcn_ds_read_tr16_b64_v4i16((LDSP s16x4*)(smem + a + 4 * 384));
      u32x2 l2 = __builtin_bit_cast(u32x2, lo), h2 = __builtin_bit_cast(u32x2, hi);
      u32x4 o = {l2.x, l2.y, h2.x, h2.y};
      *(u32x4*)(wt + ((size_t)(s * FR_S + wave_frag_off(w) + (j / 3) * 54 + ks * 3 + (j % 3)) * 64 + lane) * 16) = o;
    }
  } else {
    v -= 54;
    const int ks = v / 3, th = v % 3;
    u32x4 r[3];
#pragma unroll
    for (int i = 0; i < 3; ++i) {
      int c = tid + 256 * i; int kk = c / 24, ch = c % 24;
      int gg = kk >> 3, jj = kk & 7;
      int w = ks < 16 ? (ks >> 1) : 4 * (ks - 16) + (jj >> 1);
      int j = ks < 16 ? 4 * (ks & 1) + (jj >> 1) : 8;
      int f = 72 * w + 8 * j + 4 * (gg & 1) + 2 * (gg >> 1) + (jj & 1);
      r[i] = *(const u32x4*)(w2 + (size_t)(576 * s + f) * D + 192 * th + ch * 8);
    }
#pragma unroll
    for (int i = 0; i < 3; ++i) *(u32x4*)(smem + (tid + 256 * i) * 16) = r[i];
    __syncthreads();
#pragma unroll
    for (int i = 0; i < 3; ++i) {
      int tl = wv * 3 + i; int dt = 12 * th + tl;
      int w = dt < 20 ? dt / 5 : 4 + (dt - 20) / 4;
      int ti = dt < 20 ? dt % 5 : (dt - 20) % 4;
      int nt = dt < 20 ? 5 : 4;
      unsigned a = (8 * g + qq) * 384 + tl * 32 + pp * 8;
      s16x4 lo = __builtin_amdgcn_ds_read_tr16_b64_v4i16((LDSP s16x4*)(smem + a));
      s16x4 hi = __builtin_amdgcn_ds_read_tr16_b64_v4i16((LDSP s16x4*)(smem + a + 4 * 384));
      u32x2 l2 = __builtin_bit_cast(u32x2, lo), h2 = __builtin_bit_cast(u32x2, hi);
      u32x4 o = {l2.x, l2.y, h2.x, h2.y};
      *(u32x4*)(wt + ((size_t)(s * FR_S + wave_frag_off(w) + 162 + ks * nt + ti) * 64 + lane) * 16) = o;
    }
  }
}

DEV void kgen_block(int d, const u16* Are, const u16* Aim, const u16* Bre, const u16* Bim, const u16* Cre,
                    const u16* Cim, const u16* logdt, u16* kr, char* smem) {
  const int tid = threadIdx.x, lane = tid & 63, wv = tid >> 6, p = lane & 15, g = lane >> 4;
  const float dt = rb(expf(bf2f(logdt[d])));
  float a2[8], br[8], cr[8], ci[8];
  float mx = 0.f;
#pragma unroll
  for (int kk = 0; kk < 2; ++kk) {
    const int n0 = d * NM + 32 * wv + 16 * kk + 4 * g;
    u32x2 vare = *(const u32x2*)(Are + n0), vaim = *(const u32x2*)(Aim + n0), vbre = *(const u32x2*)(Bre + n0);
    u32x2 vbim = *(const u32x2*)(Bim + n0), vcre = *(const u32x2*)(Cre + n0), vcim = *(const u32x2*)(Cim + n0);
#pragma unroll
    for (int m = 0; m < 4; ++m) {
      auto ex = [&](const u32x2& v) { unsigned w = v[m >> 1]; return (m & 1) ? bfhi(w) : bflo(w); };
      float are = dt * ex(vare), aim = dt * ex(vaim), bre = dt * ex(vbre), bim = dt * ex(vbim), cre = ex(vcre), cim = ex(vcim);
      a2[4 * kk + m] = are * 1.44269504088896341f;
      br[4 * kk + m] = aim * 0.15915494309189535f;
      float xr = cre * bre - cim * bim, xi = cre * bim + cim * bre;
      cr[4 * kk + m] = xr; ci[4 * kk + m] = xi;
      mx = fmaxf(mx, fmaxf(fabsf(xr), fabsf(xi)));
    }
  }
  mx = fmaxf(mx, __shfl_xor(mx, 16)); mx = fmaxf(mx, __shfl_xor(mx, 32));
  int ex = (int)((__float_as_uint(mx) >> 23) & 0xff);
  ex = ex < 20 ? 20 : (ex > 230 ? 230 : ex);
  const float sc = __uint_as_float((unsigned)(127 + 12 + 127 - ex) << 23);
  const float isc = __uint_as_float((unsigned)(ex - 12) << 23);
  const float lf = (float)(8 * p);
  const float b0 = (float)((p & 7) + 128 * (p >> 3));
  const float b1 = (float)(p < 8 ? 256 + 2 * (p & 3) + 128 * (p >> 2) : 512 + 4 * (p & 1) + 128 * ((p - 8) >> 1));
  const float b2 = (float)(1024 + 128 * p);
  f32x4 acc[3] = {};
#pragma unroll
  for (int kk = 0; kk < 2; ++kk) {
    f16x8 E, X0, X1, X2;
#pragma unroll
    for (int m = 0; m < 4; ++m) {
      const float a = a2[4 * kk + m], b = br[4 * kk + m], xr = cr[4 * kk + m] * sc, xi = ci[4 * kk + m] * sc;
      auto cexp = [&](float l, float& re, float& im) {
        float e = __builtin_amdgcn_exp2f(a * l);
        float ph = __builtin_amdgcn_fractf(b * l);
        re = e * __builtin_amdgcn_cosf(ph); im = e * __builtin_amdgcn_sinf(ph);
      };
      float re, im;
      cexp(lf, re, im); E[2 * m] = (_Float16)re; E[2 * m + 1] = (_Float16)im;
      cexp(b0, re, im); X0[2 * m] = (_Float16)(xr * re - xi * im); X0[2 * m + 1] = (_Float16)(-(xr * im + xi * re));
      cexp(b1, re, im); X1[2 * m] = (_Float16)(xr * re - xi * im); X1[2 * m + 1] = (_Float16)(-(xr * im + xi * re));
      cexp(b2, re, im); X2[2 * m] = (_Float16)(xr * re - xi * im); X2[2 * m + 1] = (_Float16)(-(xr * im + xi * re));
    }
    acc[0] = __builtin_amdgcn_mfma_f32_16x16x32_f16(X0, E, acc[0], 0, 0, 0);
    acc[1] = __builtin_amdgcn_mfma_f32_16x16x32_f16(X1, E, acc[1], 0, 0, 0);
    acc[2] = __builtin_amdgcn_mfma_f32_16x16x32_f16(X2, E, acc[2], 0, 0, 0);
  }
  float* red = (float*)smem;
  float* tab = red + 4 * 768;
#pragma unroll
  for (int T = 0; T < 3; ++T)
#pragma unroll
    for (int r = 0; r < 4; ++r) red[wv * 768 + T * 256 + (4 * g + r) * 16 + p] = acc[T][r] * isc;
  __syncthreads();
#pragma unroll
  for (int i = 0; i < 3; ++i) { int x = tid + 256 * i; tab[x] = (red[x] + red[768 + x]) + (red[1536 + x] + red[2304 + x]); }
  __syncthreads();
  float kv[8];
#pragma unroll
  for (int i = 0; i < 8; ++i) {
    int l = 2047 - (8 * tid + i);
    int v = (int)rb((float)l);
    int idx;
    if (v < 256) idx = (8 * (v >> 7) + (v & 7)) * 16 + ((v >> 3) & 15);
    else if (v < 512) { int u = v - 256; idx = 256 + (4 * (u >> 7) + ((u & 7) >> 1)) * 16 + ((u >> 3) & 15); }
    else if (v < 1024) { int u = v - 512; idx = 256 + (8 + 2 * (u >> 7) + ((u & 7) >> 2)) * 16 + ((u >> 3) & 15); }
    else { int u = v - 1024; idx = 512 + (u >> 7) * 16 + ((u >> 3) & 15); }
    kv[i] = tab[idx];
  }
  u32x4 o = {pk2(kv[0], kv[1]), pk2(kv[2], kv[3]), pk2(kv[4], kv[5]), pk2(kv[6], kv[7])};
  *(u32x4*)(kr + (size_t)d * KR_STRIDE + 8 * tid) = o;
  if (tid < 2) *(u32x4*)(kr + (size_t)d * KR_STRIDE + 2048 + 8 * tid) = u32x4{0, 0, 0, 0};
}

DEV void ln1_block(int b, const u16* x, u16* xT, u16* zT, char* smem) {
  const int tid = threadIdx.x, tok = tid >> 3, c8 = tid & 7;
  const int t0 = 32 * b;
  u32x4 r[9];
#pragma unroll
  for (int i = 0; i < 9; ++i) r[i] = *(const u32x4*)(x + (size_t)(t0 + tok) * D + (c8 + 8 * i) * 8);
  float s = 0.f;
#pragma unroll
  for (int i = 0; i < 9; ++i)
#pragma unroll
    for (int e = 0; e < 8; ++e) s += elem(r[i], e);
  s += __shfl_xor(s, 1); s += __shfl_xor(s, 2); s += __shfl_xor(s, 4);
  const float mean = s * (1.f / D);
  float q = 0.f;
#pragma unroll
  for (int i = 0; i < 9; ++i)
#pragma unroll
    for (int e = 0; e < 8; ++e) { float dlt = elem(r[i], e) - mean; q += dlt * dlt; }
  q += __shfl_xor(q, 1); q += __shfl_xor(q, 2); q += __shfl_xor(q, 4);
  float* st = (float*)(smem + 36864);
  if (c8 == 0) { st[2 * tok] = rb(mean); st[2 * tok + 1] = 1.f / rb(sqrtf(rb(rb(q * (1.f / D)) + 1e-5f))); }
  const int slot = ((tok >> 3) ^ (c8 & 3)) * 16 + (tok & 7) * 2;
#pragma unroll
  for (int i = 0; i < 9; ++i)
#pragma unroll
    for (int e = 0; e < 8; ++e) {
      unsigned w = r[i][e >> 1];
      *(u16*)(smem + ((c8 + 8 * i) * 8 + e) * 64 + slot) = (u16)((e & 1) ? (w >> 16) : w);
    }
  __syncthreads();
  const int k4 = tid & 3;
  float mb[8], dn[8];
#pragma unroll
  for (int e = 0; e < 8; ++e) { mb[e] = st[2 * (8 * k4 + e)]; dn[e] = st[2 * (8 * k4 + e) + 1]; }
#pragma unroll
  for (int i = 0; i < 9; ++i) {
    int d = (tid >> 2) + 64 * i;
    u32x4 v = *(const u32x4*)(smem + d * 64 + ((k4 ^ ((d >> 3) & 3)) * 16));
    float z[8];
#pragma unroll
    for (int e = 0; e < 8; ++e) z[e] = rb(elem(v, e) - mb[e]) * dn[e];
    u32x4 o = {pk2(z[0], z[1]), pk2(z[2], z[3]), pk2(z[4], z[5]), pk2(z[6], z[7])};
    *(u32x4*)(xT + (size_t)d * L + t0 + 8 * k4) = v;
    *(u32x4*)(zT + (size_t)d * L + t0 + 8 * k4) = o;
  }
}

DEV void mod_block(int b, const u16* c, const u16* wmod, float* modp, char* smem) {
  if (b >= 54 * MODK) return;
  const int tid = threadIdx.x, c8 = tid & 7, rg = tid >> 3, lane = tid & 63, wv = tid >> 6;
  const int cg = b % 54, kq = b / 54;
  u32x4 r[6]; float sv[6];
#pragma unroll
  for (int i = 0; i < 6; ++i) {
    int k = 192 * kq + rg + 32 * i;
    r[i] = *(const u32x4*)(wmod + (size_t)k * 3456 + 64 * cg + 8 * c8);
    float cv = bf2f(c[k]);
    sv[i] = rb(cv / (1.f + expf(-cv)));
  }
  float acc[8] = {};
#pragma unroll
  for (int i = 0; i < 6; ++i)
#pragma unroll
    for (int e = 0; e < 8; ++e) acc[e] += sv[i] * elem(r[i], e);
#pragma unroll
  for (int e = 0; e < 8; ++e) { acc[e] += __shfl_xor(acc[e], 8); acc[e] += __shfl_xor(acc[e], 16); acc[e] += __shfl_xor(acc[e], 32); }
  float* red = (float*)smem;
  if (lane < 8)
#pragma unroll
    for (int e = 0; e < 8; ++e) red[wv * 64 + lane * 8 + e] = acc[e];
  __syncthreads();
  if (tid < 64) modp[kq * 3456 + 64 * cg + tid] = (red[tid] + red[64 + tid]) + (red[128 + tid] + red[192 + tid]);
}

constexpr int K1_LN = 64, K1_MOD = 168, K1_KG = 576;
constexpr int RT_K1 = 432;
__global__ __launch_bounds__(256) void k1_prep(const u16* x, const u16* Are, const u16* Aim, const u16* Bre, const u16* Bim,
    const u16* Cre, const u16* Cim, const u16* logdt, const u16* w1, const u16* w3, const u16* w2, const u16* c,
    const u16* wmod, char* ws) {
  extern __shared__ __attribute__((aligned(16))) char smem[];
  int b = blockIdx.x;
  if (b < K1_LN) { ln1_block(b, x, (u16*)(ws + WS_XT), (u16*)(ws + WS_ZT), smem); return; }
  b -= K1_LN;
  if (b < K1_MOD) { mod_block(b, c, wmod, (float*)(ws + WS_MODP), smem); return; }
  b -= K1_MOD;
  if (b < K1_KG) { kgen_block(b, Are, Aim, Bre, Bim, Cre, Cim, logdt, (u16*)(ws + WS_KR), smem); return; }
  b -= K1_KG;
  retile_unit(b, w1, w3, w2, ws + WS_WT, smem);
}

constexpr int CP_STRIDE = 4160;
constexpr int K2_U = 4 * CP_STRIDE;
constexpr int K2_RED = 0;
constexpr int K2_LDS = 4 * 2560 * 4;
__global__ __launch_bounds__(256) void k2_conv(const u16* Dskip, const u16* w1, const u16* w3, const u16* w2, char* ws) {
  extern __shared__ __attribute__((aligned(16))) char smem[];
  if (blockIdx.x >= D) { retile_unit(blockIdx.x - D + RT_K1, w1, w3, w2, ws + WS_WT, smem); return; }
  const int d = blockIdx.x, tid = threadIdx.x, lane = tid & 63, wv = tid >> 6;
  const u16* kr = (const u16*)(ws + WS_KR) + (size_t)d * KR_STRIDE;
  const u32x4 zv = *(const u32x4*)((const u16*)(ws + WS_ZT) + (size_t)d * L + 8 * tid);
  const u32x4 k0 = *(const u32x4*)(kr + 8 * tid);
  const u32x2 k1 = *(const u32x2*)(kr + 8 * tid + 8);
  const u32x4 xv = *(const u32x4*)((const u16*)(ws + WS_XT) + (size_t)d * L + 8 * tid);
  const float* mp = (const float*)(ws + WS_MODP);
  const float sa = modval(mp, d), ca1 = rb(1.f + modval(mp, D + d)), ga = modval(mp, 2 * D + d);
  const float dsk = bf2f(Dskip[d]);
  if (tid < 3) {
    float v = modval(mp, (3 + tid) * D + d);
    if (tid == 1) v = 1.f + v;
    ((u16*)(ws + WS_MODF))[tid * D + d] = (u16)pk2(v, 0.f);
  }
  {
    unsigned w0 = k0.x, w1_ = k0.y, w2_ = k0.z, w3_ = k0.w, w4 = k1.x, w5 = k1.y;
    auto al = [](unsigned hi, unsigned lo) { return __builtin_amdgcn_alignbit(hi, lo, 16); };
    *(u32x4*)(smem + 0 * CP_STRIDE + 16 * tid) = k0;
    *(u32x4*)(smem + 1 * CP_STRIDE + 16 * tid) = u32x4{al(w1_, w0), al(w2_, w1_), al(w3_, w2_), al(w4, w3_)};
    *(u32x4*)(smem + 2 * CP_STRIDE + 16 * tid) = u32x4{w1_, w2_, w3_, w4};
    *(u32x4*)(smem + 3 * CP_STRIDE + 16 * tid) = u32x4{al(w2_, w1_), al(w3_, w2_), al(w4, w3_), al(w5, w4)};
    if (tid < 8) *(u32x4*)(smem + (tid >> 1) * CP_STRIDE + 4096 + 16 * (tid & 1)) = u32x4{0, 0, 0, 0};
  }
  float h[8];
#pragma unroll
  for (int e = 0; e < 8; ++e) h[e] = rb(rb(elem(zv, e) * ca1) + sa);
  *(u32x4*)(smem + K2_U + 480 + 16 * tid) = u32x4{pk2(h[0], h[1]), pk2(h[2], h[3]), pk2(h[4], h[5]), pk2(h[6], h[7])};
  if (tid < 30) *(u32x4*)(smem + K2_U + 16 * tid) = u32x4{0, 0, 0, 0};
  __syncthreads();
  const int r = lane & 15, g = lane >> 4;
  const int sg = (3 - r) & 3;
  const unsigned abase = sg * CP_STRIDE + 2 * (2031 + 8 * g - r - sg);
  const unsigned bbase = K2_U + 2 * (240 + 16 * r - 16 + 8 * g);
  f32x4 acc[8] = {};
#pragma unroll
  for (int cc = 0; cc < 2; ++cc) {
    const int c0 = 2 * wv + cc;
    bf16x8 A[8], B[8];
#pragma unroll
    for (int i = 0; i < 8; ++i) {
      const char* pa = smem + abase - 64 * c0 - 512 * i;
      u32x2 lo = *(const u32x2*)pa, hi = *(const u32x2*)(pa + 8);
      A[i] = __builtin_bit_cast(bf16x8, u32x4{lo.x, lo.y, hi.x, hi.y});
      B[i] = *(const bf16x8*)(smem + bbase - 64 * c0 + 512 * i);
    }
#pragma unroll
    for (int c1 = 0; c1 < 8; ++c1)
#pragma unroll
      for (int i = c1; i < 8; ++i) acc[i] = __builtin_amdgcn_mfma_f32_16x16x32_bf16(A[c1], B[i - c1], acc[i], 0, 0, 0);
  }
  float* red = (float*)(smem + K2_RED);
  __syncthreads();
#pragma unroll
  for (int i = 0; i < 8; ++i) *(f32x4*)(red + wv * 2560 + (16 * i + r) * 20 + 4 * g) = acc[i];
  __syncthreads();
  float y[8];
  {
    const int o = (tid >> 1) * 20 + (tid & 1) * 8;
#pragma unroll
    for (int hh = 0; hh < 2; ++hh) {
      f32x4 a = *(const f32x4*)(red + o + 4 * hh), b = *(const f32x4*)(red + 2560 + o + 4 * hh);
      f32x4 c = *(const f32x4*)(red + 5120 + o + 4 * hh), e = *(const f32x4*)(red + 7680 + o + 4 * hh);
      f32x4 s = (a + b) + (c + e);
#pragma unroll
      for (int k = 0; k < 4; ++k) y[4 * hh + k] = s[k];
    }
  }
  float o[8];
#pragma unroll
  for (int e = 0; e < 8; ++e) { float y2 = rb(rb(y[e]) + rb(h[e] * dsk)); o[e] = elem(xv, e) + rb(ga * y2); }
  *(u32x4*)((u16*)(ws + WS_X1T) + (size_t)d * L + 8 * tid) = u32x4{pk2(o[0], o[1]), pk2(o[2], o[3]), pk2(o[4], o[5]), pk2(o[6], o[7])};
}

constexpr int K3_H = 73728, K3_ST = 2 * 73728, K3_LDS = 2 * 73728 + 4096;
constexpr int RING = 16;
DEV unsigned swiglu_tile(const f32x4& c) {
  auto s0 = __builtin_amdgcn_permlane32_swap(__float_as_uint(c[0]), __float_as_uint(c[2]), false, false);
  auto s1 = __builtin_amdgcn_permlane32_swap(__float_as_uint(c[1]), __float_as_uint(c[3]), false, false);
  float hv[2];
#pragma unroll
  for (int p = 0; p < 2; ++p) {
    float gt = rb(__uint_as_float(p ? s1[0] : s0[0])), up = rb(__uint_as_float(p ? s1[1] : s0[1]));
    float sg = gt * __builtin_amdgcn_rcpf(1.f + __builtin_amdgcn_exp2f(gt * -1.44269504088896341f));
    hv[p] = rb(sg) * up;
  }
  return pk2(hv[0], hv[1]);
}
DEV void ffn_phase1(__amdgpu_buffer_rsrc_t rs, int wv, int lane, char* smem, bf16x8 (&ring)[RING]) {
  const unsigned vo = lane * 16;
  auto ldw = [&](int idx) { return __builtin_bit_cast(bf16x8, __builtin_amdgcn_raw_buffer_load_b128(rs, vo, idx * 1024, 0)); };
  unsigned boff[4];
#pragma unroll
  for (int tt = 0; tt < 4; ++tt) boff[tt] = tt * 1024 + ((lane ^ ((2 * tt + ((lane >> 3) & 1)) & 7)) * 16);
  f32x4 acc[3][3][4];
  char* hw0 = smem + K3_H + (2 * wv) * 4096 + lane * 16;
  char* hw8 = smem + K3_H + (16 + (wv >> 2)) * 4096 + lane * 16 + (wv & 3) * 4;
  auto put_h = [&](int j, int tt, unsigned v) {
    if (j < 8) *(unsigned*)(hw0 + (j >> 2) * 4096 + tt * 1024 + (j & 3) * 4) = v;
    else *(unsigned*)(hw8 + tt * 1024) = v;
  };
  bf16x8 b[2][4];
#pragma unroll
  for (int tt = 0; tt < 4; ++tt) b[0][tt] = *(const bf16x8*)(smem + boff[tt]);
#pragma unroll
  for (int ps = 0; ps < 3; ++ps) {
#pragma unroll
    for (int ks = 0; ks < 18; ++ks) {
      if (ps < 2 || ks < 17) {
#pragma unroll
        for (int tt = 0; tt < 4; ++tt) b[(ks + 1) & 1][tt] = *(const bf16x8*)(smem + ((ks + 1) % 18) * 4096 + boff[tt]);
      }
#pragma unroll
      for (int j = 0; j < 3; ++j) {
        const int i = ps * 54 + ks * 3 + j;
#pragma unroll
        for (int tt = 0; tt < 4; ++tt)
          acc[ps][j][tt] = __builtin_amdgcn_mfma_f32_16x16x32_bf16(ring[i % RING], b[ks & 1][tt], ks == 0 ? f32x4{0.f, 0.f, 0.f, 0.f} : acc[ps][j][tt], 0, 0, 0);
        ring[i % RING] = ldw(i + RING);
        if (ps > 0 && j == 1 && ks >= 1 && ks < 13) {
          put_h(3 * (ps - 1) + (ks - 1) / 4, (ks - 1) % 4, swiglu_tile(acc[ps - 1][(ks - 1) / 4][(ks - 1) % 4]));
        }
        __builtin_amdgcn_sched_barrier(0);
      }
    }
  }
#pragma unroll
  for (int j = 0; j < 3; ++j)
#pragma unroll
    for (int tt = 0; tt < 4; ++tt) put_h(6 + j, tt, swiglu_tile(acc[2][j][tt]));
  __syncthreads();
}
template <int NT>
DEV void ffn_phase2(__amdgpu_buffer_rsrc_t rs, int wv, int lane, char* smem, char* pout, bf16x8 (&ring)[RING]) {
  const unsigned vo = lane * 16;
  constexpr int NFR = 162 + 18 * NT;
  auto ldw = [&](int idx) { return __builtin_bit_cast(bf16x8, __builtin_amdgcn_raw_buffer_load_b128(rs, vo, idx * 1024, 0)); };
  bf16x8 b[2][4];
  f32x4 oc[NT][4];
#pragma unroll
  for (int tt = 0; tt < 4; ++tt) b[0][tt] = *(const bf16x8*)(smem + K3_H + tt * 1024 + lane * 16);
#pragma unroll
  for (int ks = 0; ks < 18; ++ks) {
    if (ks < 17) {
#pragma unroll
      for (int tt = 0; tt < 4; ++tt) b[(ks + 1) & 1][tt] = *(const bf16x8*)(smem + K3_H + (ks + 1) * 4096 + tt * 1024 + lane * 16);
    }
#pragma unroll
    for (int ti = 0; ti < NT; ++ti) {
      const int i = 162 + ks * NT + ti;
#pragma unroll
      for (int tt = 0; tt < 4; ++tt)
        oc[ti][tt] = __builtin_amdgcn_mfma_f32_16x16x32_bf16(ring[i % RING], b[ks & 1][tt], ks == 0 ? f32x4{0.f, 0.f, 0.f, 0.f} : oc[ti][tt], 0, 0, 0);
      if (i + RING < NFR) ring[i % RING] = ldw(i + RING);
      __builtin_amdgcn_sched_barrier(0);
    }
  }
  const int dt0 = NT == 5 ? 5 * wv : 20 + 4 * (wv - 4);
#pragma unroll
  for (int ti = 0; ti < NT; ++ti)
#pragma unroll
    for (int tp = 0; tp < 2; ++tp) {
      const f32x4 u = oc[ti][2 * tp], v = oc[ti][2 * tp + 1];
      *(u32x4*)(pout + (((dt0 + ti) * 2 + tp) * 64 + lane) * 16) = u32x4{pk2(u[0], u[1]), pk2(u[2], u[3]), pk2(v[0], v[1]), pk2(v[2], v[3])};
    }
}

__global__ __launch_bounds__(512, 2) void k3_ffn(char* ws) {
  extern __shared__ __attribute__((aligned(16))) char smem[];
  const int tid = threadIdx.x, lane = tid & 63;
  const int wv = __builtin_amdgcn_readfirstlane(tid >> 6);
  const int s = blockIdx.x & 7, mt = blockIdx.x >> 3;
  const u16* x1T = (const u16*)(ws + WS_X1T) + 64 * mt;
  const u16* mf = (const u16*)(ws + WS_MODF);
  const int tc = tid & 7, cg = tid >> 3;
  u32x4 r[9];
#pragma unroll
  for (int e = 0; e < 8; ++e) r[e] = *(const u32x4*)(x1T + (size_t)(8 * cg + e) * L + 8 * tc);
  r[8] = *(const u32x4*)(x1T + (size_t)(512 + cg) * L + 8 * tc);
  const u32x4 sbv = *(const u32x4*)(mf + 8 * cg), cbv = *(const u32x4*)(mf + D + 8 * cg);
  const float sb8 = bf2f(mf[512 + cg]), cb8 = bf2f(mf[D + 512 + cg]);
  __builtin_amdgcn_sched_barrier(0);
  char* wbase = ws + WS_WT + ((size_t)s * FR_S + wave_frag_off(wv)) * 1024;
  __amdgpu_buffer_rsrc_t rs = __builtin_amdgcn_make_buffer_rsrc(wbase, 0, 252 * 1024, 0x00020000);
  bf16x8 ring[RING];
#pragma unroll
  for (int i = 0; i < RING; ++i) ring[i] = __builtin_bit_cast(bf16x8, __builtin_amdgcn_raw_buffer_load_b128(rs, lane * 16, i * 1024, 0));
  __builtin_amdgcn_sched_barrier(0);
  float s1[8] = {}, s2[8] = {};
#pragma unroll
  for (int e = 0; e < 9; ++e)
#pragma unroll
    for (int t = 0; t < 8; ++t) { float v = elem(r[e], t); s1[t] += v; s2[t] += v * v; }
  {
    const bool b5 = lane & 32, b4 = lane & 16, b3 = lane & 8;
#pragma unroll
    for (int t = 0; t < 4; ++t) {
      float k1 = b5 ? s1[t + 4] : s1[t], o1 = b5 ? s1[t] : s1[t + 4];
      float k2 = b5 ? s2[t + 4] : s2[t], o2 = b5 ? s2[t] : s2[t + 4];
      s1[t] = k1 + __shfl_xor(o1, 32); s2[t] = k2 + __shfl_xor(o2, 32);
    }
#pragma unroll
    for (int t = 0; t < 2; ++t) {
      float k1 = b4 ? s1[t + 2] : s1[t], o1 = b4 ? s1[t] : s1[t + 2];
      float k2 = b4 ? s2[t + 2] : s2[t], o2 = b4 ? s2[t] : s2[t + 2];
      s1[t] = k1 + __shfl_xor(o1, 16); s2[t] = k2 + __shfl_xor(o2, 16);
    }
    float k1 = b3 ? s1[1] : s1[0], o1 = b3 ? s1[0] : s1[1];
    float k2 = b3 ? s2[1] : s2[0], o2 = b3 ? s2[0] : s2[1];
    s1[0] = k1 + __shfl_xor(o1, 8); s2[0] = k2 + __shfl_xor(o2, 8);
  }
  float* st = (float*)(smem + K3_ST);
  *(f32x2*)(st + (wv * 64 + 8 * tc + (lane >> 3)) * 2) = f32x2{s1[0], s2[0]};
  __syncthreads();
  {
    float a = 0.f, b = 0.f;
#pragma unroll
    for (int w = 0; w < 8; ++w) { f32x2 v = *(const f32x2*)(st + (w * 64 + lane) * 2); a += v.x; b += v.y; }
    const float mean = a * (1.f / D);
    const float var = fmaxf(b * (1.f / D) - mean * mean, 0.f);
    s1[0] = rb(mean); s2[0] = 1.f / rb(sqrtf(rb(rb(var) + 1e-5f)));
  }
  char* base = smem + (cg >> 2) * 4096 + (tc >> 1) * 1024 + (cg & 3) * 256 + (tc & 1) * 128;
  char* base8 = smem + (16 + (wv >> 2)) * 4096 + (tc >> 1) * 1024 + (wv & 3) * 256 + (tc & 1) * 128 + (cg & 7) * 2;
#pragma unroll
  for (int t = 0; t < 8; ++t) {
    const float mb = __shfl(s1[0], 8 * tc + t), rd = __shfl(s2[0], 8 * tc + t);
    float hv[8];
#pragma unroll
    for (int e = 0; e < 8; ++e) hv[e] = rb(rb(rb(elem(r[e], t) - mb) * rd) * elem(cbv, e)) + elem(sbv, e);
    *(u32x4*)(base + ((t ^ tc) * 16)) = u32x4{pk2(hv[0], hv[1]), pk2(hv[2], hv[3]), pk2(hv[4], hv[5]), pk2(hv[6], hv[7])};
    const float h8 = rb(rb(rb(elem(r[8], t) - mb) * rd) * cb8) + sb8;
    *(u16*)(base8 + ((t ^ tc) * 16)) = (u16)pk2(h8, 0.f);
  }
  char* pout = ws + WS_P + ((size_t)mt * 8 + s) * 73728;
  __syncthreads();
  ffn_phase1(rs, wv, lane, smem, ring);
  if (wv < 4) { ffn_phase2<5>(rs, wv, lane, smem, pout, ring); __builtin_amdgcn_endpgm(); }
  ffn_phase2<4>(rs, wv, lane, smem, pout, ring);
}

__global__ __launch_bounds__(256) void k4_out(const char* ws, u16* out) {
  const int gid = blockIdx.x * 256 + threadIdx.x;
  const int mt = gid / 4608, rem = gid % 4608, lane = rem & 63, tp = (rem >> 6) & 1, dt = rem >> 7;
  const char* p = ws + WS_P + (size_t)mt * 8 * 73728 + (size_t)rem * 16;
  u32x4 v[8];
#pragma unroll
  for (int s = 0; s < 8; ++s) v[s] = *(const u32x4*)(p + (size_t)s * 73728);
  const int d0 = 16 * dt + 4 * (lane >> 4), t0 = 64 * mt + 32 * tp + (lane & 15);
  const u16* x1T = (const u16*)(ws + WS_X1T);
  const u16* mf = (const u16*)(ws + WS_MODF);
  float xr[8], gb[4];
#pragma unroll
  for (int e = 0; e < 4; ++e) { xr[e] = bf2f(x1T[(size_t)(d0 + e) * L + t0]); xr[4 + e] = bf2f(x1T[(size_t)(d0 + e) * L + t0 + 16]); gb[e] = bf2f(mf[2 * D + d0 + e]); }
  float o[8];
#pragma unroll
  for (int e = 0; e < 8; ++e) {
    float a = ((elem(v[0], e) + elem(v[1], e)) + (elem(v[2], e) + elem(v[3], e))) + ((elem(v[4], e) + elem(v[5], e)) + (elem(v[6], e) + elem(v[7], e)));
    o[e] = xr[e] + rb(gb[e & 3] * rb(a));
  }
  *(u32x2*)(out + (size_t)t0 * D + d0) = u32x2{pk2(o[0], o[1]), pk2(o[2], o[3])};
  *(u32x2*)(out + (size_t)(t0 + 16) * D + d0) = u32x2{pk2(o[4], o[5]), pk2(o[6], o[7])};
}

extern "C" void kernel_launch(void* const* d_in, const int* in_sizes, int n_in, void* d_out, int out_size, void* d_ws,
                              size_t ws_size, hipStream_t stream) {
  const u16* x = (const u16*)d_in[0];
  const u16 *Are = (const u16*)d_in[1], *Aim = (const u16*)d_in[2], *Bre = (const u16*)d_in[3], *Bim = (const u16*)d_in[4];
  const u16 *Cre = (const u16*)d_in[5], *Cim = (const u16*)d_in[6], *logdt = (const u16*)d_in[7], *Dskip = (const u16*)d_in[8];
  const u16 *w1 = (const u16*)d_in[10], *w3 = (const u16*)d_in[11], *w2 = (const u16*)d_in[12];
  const u16 *c = (const u16*)d_in[13], *wmod = (const u16*)d_in[14];
  char* ws = (char*)d_ws;
  (void)hipFuncSetAttribute((const void*)k3_ffn, hipFuncAttributeMaxDynamicSharedMemorySize, K3_LDS);
  k1_prep<<<K1_LN + K1_MOD + K1_KG + RT_K1, 256, 36864 + 256, stream>>>(x, Are, Aim, Bre, Bim, Cre, Cim, logdt, w1, w3, w2, c, wmod, ws);
  k2_conv<<<D + (864 - RT_K1), 256, K2_LDS, stream>>>(Dskip, w1, w3, w2, ws);
  k3_ffn<<<256, 512, K3_LDS, stream>>>(ws);
  k4_out<<<576, 256, 0, stream>>>(ws, (u16*)d_out);
}
```

```cpp
#include <hip/hip_runtime.h>
#include <hip/hip_bf16.h>
#include <stdint.h>

typedef __attribute__((ext_vector_type(8))) __bf16 bf16x8;
typedef __attribute__((ext_vector_type(2))) __bf16 bf16x2;
typedef __attribute__((ext_vector_type(8))) _Float16 f16x8;
typedef __attribute__((ext_vector_type(4))) float f32x4;
typedef __attribute__((ext_vector_type(2))) float f32x2;
typedef __attribute__((ext_vector_type(4))) unsigned u32x4;
typedef __attribute__((ext_vector_type(2))) unsigned u32x2;
typedef __attribute__((ext_vector_type(4))) short s16x4;
typedef unsigned short u16;
#define LDSP __attribute__((address_space(3)))
#define DEV __device__ __forceinline__

#define STAMP(i)
#define STAMP_DECL
#define STAMP_ARG
#define STAMP_PASS
#define STAMP_OUT(ws, n)
#define KT_BEGIN(k)
#define KT_END(k, ws)
#define K2PRIO 1
#define PREF1 1
#define LBAR 0
#define PREF 0
#define PREF2 0
#define SPX 2
constexpr int L = 2048, D = 576, NM = 128, F = 4608;
constexpr int KR_STRIDE = 2064;
constexpr int MODK = 4;
constexpr int FR_S = 144 + 664;

constexpr size_t WS_MODP = 0;
constexpr size_t WS_MODF = 57344;
constexpr size_t WS_XT   = 65536;
constexpr size_t WS_ZT   = WS_XT + (size_t)D * L * 2;
constexpr size_t WS_X1T  = WS_ZT + (size_t)D * L * 2;
constexpr size_t WS_KR   = WS_X1T + (size_t)D * L * 2;
constexpr size_t WS_WT   = (WS_KR + (size_t)D * KR_STRIDE * 2 + 4095) / 4096 * 4096;
constexpr size_t WS_P    = WS_WT + (size_t)8 * FR_S * 1024;

DEV float bf2f(unsigned v) { return __uint_as_float(v << 16); }
DEV float bflo(unsigned v) { return __uint_as_float(v << 16); }
DEV float bfhi(unsigned v) { return __uint_as_float(v & 0xffff0000u); }
DEV float rb(float x) { return (float)(__bf16)x; }
DEV unsigned pk2(float a, float b) { f32x2 v = {a, b}; bf16x2 r = __builtin_convertvector(v, bf16x2); return __builtin_bit_cast(unsigned, r); }
DEV float elem(const u32x4& v, int i) { unsigned w = v[i >> 1]; return (i & 1) ? bfhi(w) : bflo(w); }
DEV int wave_frag_off(int w) { return 144 - 16 + (w < 4 ? 110 * w : 440 + 56 * (w - 4)); }
DEV int frag_pos(int s, int w, int i) { return s * FR_S + (i < 16 ? i * 9 + w : wave_frag_off(w) + i); }

DEV void lds_barrier() { asm volatile("s_waitcnt lgkmcnt(0)" ::: "memory"); __builtin_amdgcn_s_barrier(); asm volatile("" ::: "memory"); }
DEV void retile1_load(int u, const u16* w1, const u16* w3, u32x4 (&r)[3]) {
  const int tid = threadIdx.x & 255, s = u & 7, ks = u >> 3;
  const size_t o = (size_t)(32 * ks + (tid >> 3)) * F + 576 * s + 512 + (tid & 7) * 8;
  r[0] = *(const u32x4*)(w1 + o);
  r[1] = *(const u32x4*)(w3 + o);
}
DEV void retile2_load(int u, const u16* w2, u32x4 (&r)[3]) {
  const int tid = threadIdx.x & 255, s = u & 7, v = (u >> 3) - 18, ks = v / 3, th = v % 3;
#pragma unroll
  for (int i = 0; i < 3; ++i) {
    const int c = tid + 256 * i, kk = c / 24, ch = c % 24, gg = kk >> 3, jj = kk & 7;
    const int f = ks < 16 ? 64 * (ks >> 1) + 8 * (4 * (gg & 1) + 2 * (gg >> 1) + (jj & 1)) + 4 * (ks & 1) + (jj >> 1)
                          : 512 + 32 * (ks - 16) + 8 * (jj >> 1) + 4 * (gg & 1) + 2 * (gg >> 1) + (jj & 1);
    r[i] = *(const u32x4*)(w2 + (size_t)(576 * s + f) * D + 192 * th + ch * 8);
  }
}
template <int N> DEV void retile_stage(const u32x4 (&r)[3], char* st) {
#pragma unroll
  for (int i = 0; i < N; ++i) *(u32x4*)(st + ((threadIdx.x & 255) + 256 * i) * 16) = r[i];
}
template <int RS> DEV u32x4 tr_frag(const char* p) {
  s16x4 lo = __builtin_amdgcn_ds_read_tr16_b64_v4i16((LDSP s16x4*)p);
  s16x4 hi = __builtin_amdgcn_ds_read_tr16_b64_v4i16((LDSP s16x4*)(p + 4 * RS));
  u32x2 l2 = __builtin_bit_cast(u32x2, lo), h2 = __builtin_bit_cast(u32x2, hi);
  return u32x4{l2.x, l2.y, h2.x, h2.y};
}
DEV void retile1_emit(int u, const char* st, char* wt) {
  const int lane = threadIdx.x & 63, wv = __builtin_amdgcn_readfirstlane((threadIdx.x >> 6) & 3);
  const int s = u & 7, ks = u >> 3;
  const int g = lane >> 4, qq = (lane & 15) >> 2, pp = lane & 3;
  const char* p = st + (pp >> 1) * 4096 + (8 * g + qq) * 128 + (pp & 1) * 8 + wv * 32;
#pragma unroll
  for (int i = 0; i < 2; ++i)
    *(u32x4*)(wt + (size_t)frag_pos(s, wv, 2 * ks + i) * 1024 + lane * 16) = tr_frag<128>(p + i * 16);
}
DEV void retile2_emit(int u, const char* st, char* wt) {
  const int lane = threadIdx.x & 63, wv = __builtin_amdgcn_readfirstlane((threadIdx.x >> 6) & 3);
  const int s = u & 7, v = (u >> 3) - 18, ks = v / 3, th = v % 3;
  const int g = lane >> 4, qq = (lane & 15) >> 2, pp = lane & 3;
  const char* p = st + (8 * g + qq) * 384 + pp * 8 + wv * 96;
#pragma unroll
  for (int i = 0; i < 3; ++i) {
    const int dt = 12 * th + wv * 3 + i;
    const int w = dt < 20 ? dt / 5 : 4 + (dt - 20) / 4, ti = dt < 20 ? dt % 5 : (dt - 20) % 4, nt = dt < 20 ? 5 : 4;
    *(u32x4*)(wt + (size_t)frag_pos(s, w, (dt < 20 ? 36 : 0) + ks * nt + ti) * 1024 + lane * 16) = tr_frag<384>(p + i * 32);
  }
}

#define RT_K1 144
struct cf { float re, im; };
DEV cf cmul(cf a, cf b) { return cf{a.re * b.re - a.im * b.im, a.re * b.im + a.im * b.re}; }
constexpr int KG_RS = 528;
constexpr int K1_G0 = 16 * (1152 + 16), KG_TAB = 0, KG_OUT = KG_TAB + 57 * KG_RS, KG_SC = KG_OUT + 660 * 4, K1_G = KG_SC + 16;
DEV void kgen_block(int d, const u16* Are, const u16* Aim, const u16* Bre, const u16* Bim, const u16* Cre,
                    const u16* Cim, const u16* logdt, const u16* w1, const u16* w3, const u16* w2, char* ws, char* smem) {
  const int tid = threadIdx.x & 255, lane = tid & 63, wv = __builtin_amdgcn_readfirstlane(tid >> 6), p = lane & 15, g = lane >> 4;
  float* tab = (float*)(smem + KG_OUT);
  float* scl = (float*)(smem + KG_SC);
  STAMP_DECL
  STAMP(0);
  const int mode = tid & 127, n = d * NM + mode;
  const unsigned iare = Are[n], iaim = Aim[n], ibre = Bre[n], ibim = Bim[n], icre = Cre[n], icim = Cim[n], ildt = logdt[d];
  STAMP(1);
  unsigned pf0 = 0;
  {
    const float dt = rb(expf(bf2f(ildt)));
    const float a2 = dt * bf2f(iare) * 1.44269504088896341f, br = dt * bf2f(iaim) * 0.15915494309189535f;
    cf cb = cmul(cf{bf2f(icre), bf2f(icim)}, cf{dt * bf2f(ibre), dt * bf2f(ibim)});
    if (wv < 2) {
      unsigned off = (576 * (lane >> 3) + 64 * (lane & 7)) * 2;
      asm volatile("" : "+v"(off) : "v"(a2), "v"(br), "v"(cb.re));
      pf0 = *(const unsigned*)((const char*)((wv ? w3 : w1) + (size_t)d * F) + off);
    }
    float mx = fmaxf(fabsf(cb.re), fabsf(cb.im));
    mx = fmaxf(mx, __int_as_float(__builtin_amdgcn_update_dpp(0, __float_as_int(mx), 0xB1, 0xf, 0xf, false)));
    mx = fmaxf(mx, __int_as_float(__builtin_amdgcn_update_dpp(0, __float_as_int(mx), 0x4E, 0xf, 0xf, false)));
    mx = fmaxf(mx, __int_as_float(__builtin_amdgcn_update_dpp(0, __float_as_int(mx), 0x124, 0xf, 0xf, false)));
    mx = fmaxf(mx, __int_as_float(__builtin_amdgcn_update_dpp(0, __float_as_int(mx), 0x128, 0xf, 0xf, false)));
    { auto q = __builtin_amdgcn_permlane16_swap(__float_as_uint(mx), __float_as_uint(mx), false, false); mx = fmaxf(__uint_as_float(q[0]), __uint_as_float(q[1])); }
    int ex = (int)((__float_as_uint(mx) >> 23) & 0xff);
    ex = ex < 20 ? 20 : (ex > 230 ? 230 : ex);
    const float sc = __uint_as_float((unsigned)(127 + 12 + 127 - ex) << 23);
    cb.re *= sc; cb.im *= sc;
    if (wv < 2 && (lane & 31) == 0) scl[mode >> 5] = __uint_as_float((unsigned)(ex - 12) << 23);
    auto cexp = [&](float l) {
      const float e = __builtin_amdgcn_exp2f(a2 * l), ph = __builtin_amdgcn_fractf(br * l);
      return cf{e * __builtin_amdgcn_cosf(ph), e * __builtin_amdgcn_sinf(ph)};
    };
    char* trow = smem + KG_TAB + mode * 4;
    auto put = [&](int row, float re, float im) {
      typedef __attribute__((ext_vector_type(2))) _Float16 f16x2;
      *(f16x2*)(trow + row * KG_RS) = __builtin_convertvector(f32x2{re, im}, f16x2);
    };
    const cf w128 = cexp(128.f);
    if (wv < 2) {
      const cf w1_ = cexp(1.f), w8 = cexp(8.f), w64 = cexp(64.f);
      cf e0 = {1.f, 0.f}, e1 = w64, x = cb;
#pragma unroll
      for (int j = 0; j < 8; ++j) {
        put(j, e0.re, -e0.im); put(8 + j, e1.re, -e1.im);
        const cf y = cmul(x, w128);
        put(16 + j, x.re, x.im); put(24 + j, y.re, y.im);
        if (j < 7) { e0 = cmul(e0, w8); e1 = cmul(e1, w8); x = cmul(x, w1_); }
      }
    } else {
      const cf w2_ = cexp(2.f), w4 = cmul(w2_, w2_);
      cf x = cmul(cb, cexp(256.f));
#pragma unroll
      for (int j = 0; j < 4; ++j) {
        const cf y = cmul(x, w128);
        put(32 + j, x.re, x.im); put(36 + j, y.re, y.im);
        if (j < 3) x = cmul(x, w2_);
      }
      x = cmul(cb, cexp(512.f));
#pragma unroll
      for (int j = 0; j < 4; ++j) {
        const cf y = cmul(x, w4);
        put(40 + 2 * j, x.re, x.im); put(41 + 2 * j, y.re, y.im);
        if (j < 3) x = cmul(x, w128);
      }
      x = cmul(cb, cexp(1024.f));
      cf z = cmul(cb, cexp(1536.f));
#pragma unroll
      for (int j = 0; j < 5; ++j) {
        if (j < 4) put(48 + j, x.re, x.im);
        put(52 + j, z.re, z.im);
        if (j < 4) { x = cmul(x, w128); z = cmul(z, w128); }
      }
    }
  }
  STAMP(2);
  lds_barrier();
  STAMP(3);
  if (wv < 3) {
    const int arow = 16 + 16 * wv + (wv == 2 && p > 8 ? 8 : p);
    f32x4 sum = {0.f, 0.f, 0.f, 0.f};
#pragma unroll
    for (int gq = 0; gq < 4; ++gq) {
      f32x4 acc = {0.f, 0.f, 0.f, 0.f};
#pragma unroll
      for (int kk = 0; kk < 2; ++kk) {
        const char* q = smem + KG_TAB + (32 * gq + 16 * kk + 4 * g) * 4;
        acc = __builtin_amdgcn_mfma_f32_16x16x32_f16(*(const f16x8*)(q + arow * KG_RS), *(const f16x8*)(q + p * KG_RS), acc, 0, 0, 0);
      }
      sum += acc * scl[gq];
    }
#pragma unroll
    for (int r = 0; r < 4; ++r) {
      const int row = 4 * g + r;
      int idx;
      if (wv == 0) idx = (row & 7) + 128 * (row >> 3) + 8 * p;
      else if (wv == 1) idx = row < 8 ? 256 + (row & 3) + 64 * (row >> 2) + 4 * p : 384 + (row & 1) + 32 * ((row - 8) >> 1) + 2 * p;
      else idx = row < 9 ? 512 + 16 * row + p : 656;
      tab[idx] = sum[r];
    }
  }
  STAMP(4); STAMP(5); STAMP(6);
  lds_barrier();
  STAMP(7);
  float kv[8];
#pragma unroll
  for (int i = 0; i < 8; i += 2) {
    const int l = 2047 - (8 * tid + i);
    const unsigned bits = pk2((float)l, (float)(l - 1));
    kv[i] = tab[l < 128 ? l : (int)(bits & 0xffff) - 17024];
    kv[i + 1] = tab[l - 1 < 128 ? l - 1 : (int)(bits >> 16) - 17024];
  }
  u16* kr = (u16*)(ws + WS_KR) + (size_t)d * KR_STRIDE;
  *(u32x4*)(kr + 8 * tid) = u32x4{pk2(kv[0], kv[1]), pk2(kv[2], kv[3]), pk2(kv[4], kv[5]), pk2(kv[6], kv[7])};
  if (tid < 2) *(u32x4*)(kr + 2048 + 8 * tid) = u32x4{0, 0, 0, 0};
  asm volatile("" :: "v"(pf0));
  STAMP(8);
}

constexpr int LN_RS = 1152 + 16;
DEV void ln1_block(int u, const u16* x, u16* xT, float* stat, char* smem) {
  const int tid = threadIdx.x & 255, tok = tid >> 4, c16 = tid & 15, lane = tid & 63, wv = __builtin_amdgcn_readfirstlane(tid >> 6);
  const int t0 = 16 * ((u & 7) * 16 + (u >> 3));
  const u16* xr = x + (size_t)(t0 + tok) * D + c16 * 8;
  u32x4 r[5];
#pragma unroll
  for (int i = 0; i < 4; ++i) r[i] = *(const u32x4*)(xr + 128 * i);
  r[4] = c16 < 8 ? *(const u32x4*)(xr + 512) : u32x4{0, 0, 0, 0};
#pragma unroll
  for (int i = 0; i < 4; ++i) *(u32x4*)(smem + tok * LN_RS + (c16 + 16 * i) * 16) = r[i];
  if (c16 < 8) *(u32x4*)(smem + tok * LN_RS + (c16 + 64) * 16) = r[4];
  f32x2 sp = {0.f, 0.f};
#pragma unroll
  for (int i = 0; i < 5; ++i)
#pragma unroll
    for (int e = 0; e < 4; ++e) sp += f32x2{bflo(r[i][e]), bfhi(r[i][e])};
  auto sum16 = [](float v) {
    v += __int_as_float(__builtin_amdgcn_update_dpp(0, __float_as_int(v), 0xB1, 0xf, 0xf, false));
    v += __int_as_float(__builtin_amdgcn_update_dpp(0, __float_as_int(v), 0x4E, 0xf, 0xf, false));
    v += __int_as_float(__builtin_amdgcn_update_dpp(0, __float_as_int(v), 0x124, 0xf, 0xf, false));
    v += __int_as_float(__builtin_amdgcn_update_dpp(0, __float_as_int(v), 0x128, 0xf, 0xf, false));
    return v;
  };
  const float mean = sum16(sp.x + sp.y) * (1.f / D);
  f32x2 qp = {0.f, 0.f};
#pragma unroll
  for (int i = 0; i < 5; ++i)
#pragma unroll
    for (int e = 0; e < 4; ++e) {
      f32x2 dl = f32x2{bflo(r[i][e]), bfhi(r[i][e])} - mean;
      if (i == 4 && c16 >= 8) dl = f32x2{0.f, 0.f};
      qp += dl * dl;
    }
  const float q = sum16(qp.x + qp.y);
  if (c16 == 0) { stat[t0 + tok] = rb(mean); stat[L + t0 + tok] = 1.f / rb(sqrtf(rb(rb(q * (1.f / D)) + 1e-5f))); }
  __syncthreads();
  const int g = lane >> 4, qq = (lane & 15) >> 2, pp = lane & 3;
  const char* p = smem + (8 * (g & 1) + qq) * LN_RS + pp * 8 + (g >> 1) * 32 + wv * 64;
  u16* o = xT + (size_t)(32 * wv + 16 * (g >> 1) + (lane & 15)) * L + t0 + 8 * (g & 1);
#pragma unroll
  for (int i = 0; i < 5; ++i)
    if (i < 4 || wv < 2) *(u32x4*)(o + (size_t)i * 128 * L) = tr_frag<LN_RS>(p + i * 256);
}

DEV void mod_block(int b, const u16* c, const u16* wmod, float* modp, char* smem) {
  const int tid = threadIdx.x & 255, c16 = tid & 15, rg = tid >> 4, lane = tid & 63, wv = tid >> 6;
  const int cg = b % 27, kq = b / 27;
  u32x4 r[9]; unsigned cv[9];
#pragma unroll
  for (int i = 0; i < 9; ++i) {
    const int k = 144 * kq + rg + 16 * i;
    r[i] = *(const u32x4*)(wmod + (size_t)k * 3456 + 128 * cg + 8 * c16);
    cv[i] = c[k];
  }
  float acc[8] = {};
#pragma unroll
  for (int i = 0; i < 9; ++i) {
    const float x = bf2f(cv[i]);
    const float sv = rb(x / (1.f + __expf(-x)));
#pragma unroll
    for (int e = 0; e < 8; ++e) acc[e] += sv * elem(r[i], e);
  }
#pragma unroll
  for (int e = 0; e < 8; ++e) { acc[e] += __shfl_xor(acc[e], 16); acc[e] += __shfl_xor(acc[e], 32); }
  float* red = (float*)smem;
  if (lane < 16)
#pragma unroll
    for (int e = 0; e < 8; ++e) red[wv * 128 + lane * 8 + e] = acc[e];
  __syncthreads();
  if (tid < 128) modp[kq * 3456 + 128 * cg + tid] = (red[tid] + red[128 + tid]) + (red[256 + tid] + red[384 + tid]);
}

constexpr int K1_LDS = K1_G0 + 3 * 36864;
__global__ __launch_bounds__(1024) void k1_prep(const u16* x, const u16* Are, const u16* Aim, const u16* Bre, const u16* Bim,
    const u16* Cre, const u16* Cim, const u16* logdt, const u16* w1, const u16* w3, const u16* w2, const u16* c,
    const u16* wmod, char* ws) {
  extern __shared__ __attribute__((aligned(16))) char smem[];
  KT_BEGIN(1)
  const int b = blockIdx.x, grp = __builtin_amdgcn_readfirstlane(threadIdx.x >> 8);
  if (grp == 0) {
    if (b < 128) ln1_block(b, x, (u16*)(ws + WS_XT), (float*)(ws + WS_ZT), smem);
    else if (b < 128 + 27 * MODK) mod_block(b - 128, c, wmod, (float*)(ws + WS_MODP), smem);
  } else if (b < 192) {
    kgen_block(b + 192 * (grp - 1), Are, Aim, Bre, Bim, Cre, Cim, logdt, w1, w3, w2, ws, smem + K1_G0 + (grp - 1) * K1_G);
  } else {
    const int u0 = (b - 192) + 64 * (grp - 1);
    char* st = smem + K1_G0 + (grp - 1) * 36864;
    u32x4 r0[3], r1[3], r2[3];
    if (u0 < 144) retile1_load(u0, w1, w3, r0); else retile2_load(u0, w2, r0);
    retile2_load(u0 + 192, w2, r1);
    retile2_load(u0 + 384, w2, r2);
    if (u0 < 144) retile_stage<2>(r0, st); else retile_stage<3>(r0, st);
    retile_stage<3>(r1, st + 12288);
    retile_stage<3>(r2, st + 24576);
    __syncthreads();
    if (u0 < 144) retile1_emit(u0, st, ws + WS_WT); else retile2_emit(u0, st, ws + WS_WT);
    retile2_emit(u0 + 192, st + 12288, ws + WS_WT);
    retile2_emit(u0 + 384, st + 24576, ws + WS_WT);
  }
  KT_END(1, ws);
}

constexpr int CP_STRIDE = 4160;
constexpr int K2_U = 4 * CP_STRIDE;
constexpr int K2_RED = 0;
constexpr int K2_LDS = 4 * 2560 * 4;
__global__ __launch_bounds__(256, 3) void k2_conv(const u16* Dskip, const u16* w1, const u16* w3, const u16* w2, char* ws) {
  extern __shared__ __attribute__((aligned(16))) char smem[];
  KT_BEGIN(2)
  STAMP_DECL
  STAMP(0);
  const int d = blockIdx.x, tid = threadIdx.x, lane = tid & 63, wv = tid >> 6;
  if (d < 256) __builtin_amdgcn_s_setprio(2); else if (d < 512) __builtin_amdgcn_s_setprio(1);
  const u16* kr = (const u16*)(ws + WS_KR) + (size_t)d * KR_STRIDE;
  const u32x4 xv = *(const u32x4*)((const u16*)(ws + WS_XT) + (size_t)d * L + 8 * tid);
  const float* stat = (const float*)(ws + WS_ZT) + 8 * tid;
  const f32x4 m0 = *(const f32x4*)stat, m1 = *(const f32x4*)(stat + 4), r0 = *(const f32x4*)(stat + L), r1 = *(const f32x4*)(stat + L + 4);
  const u32x4 k0 = *(const u32x4*)(kr + 8 * tid);
  const u32x2 k1 = *(const u32x2*)(kr + 8 * tid + 8);
  const float* mp = (const float*)(ws + WS_MODP) + d;
  float mv[6][4];
#pragma unroll
  for (int i = 0; i < 6; ++i)
#pragma unroll
    for (int k = 0; k < 4; ++k) mv[i][k] = mp[k * 3456 + i * D];
  const float dsk = bf2f(Dskip[d]);
  const float sa = rb((mv[0][0] + mv[0][1]) + (mv[0][2] + mv[0][3])), ca1 = rb(1.f + rb((mv[1][0] + mv[1][1]) + (mv[1][2] + mv[1][3]))), ga = rb((mv[2][0] + mv[2][1]) + (mv[2][2] + mv[2][3]));
  STAMP(1);
  {
    unsigned w0 = k0.x, w1_ = k0.y, w2_ = k0.z, w3_ = k0.w, w4 = k1.x, w5 = k1.y;
    auto al = [](unsigned hi, unsigned lo) { return __builtin_amdgcn_alignbit(hi, lo, 16); };
    *(u32x4*)(smem + 0 * CP_STRIDE + 16 * tid) = k0;
    *(u32x4*)(smem + 1 * CP_STRIDE + 16 * tid) = u32x4{al(w1_, w0), al(w2_, w1_), al(w3_, w2_), al(w4, w3_)};
    *(u32x4*)(smem + 2 * CP_STRIDE + 16 * tid) = u32x4{w1_, w2_, w3_, w4};
    *(u32x4*)(smem + 3 * CP_STRIDE + 16 * tid) = u32x4{al(w2_, w1_), al(w3_, w2_), al(w4, w3_), al(w5, w4)};
    if (tid < 8) *(u32x4*)(smem + (tid >> 1) * CP_STRIDE + 4096 + 16 * (tid & 1)) = u32x4{0, 0, 0, 0};
  }
  float h[8];
#pragma unroll
  for (int e = 0; e < 8; ++e) h[e] = rb(rb(rb(rb(elem(xv, e) - (e < 4 ? m0[e & 3] : m1[e & 3])) * (e < 4 ? r0[e & 3] : r1[e & 3])) * ca1) + sa);
  *(u32x4*)(smem + K2_U + 480 + 16 * tid) = u32x4{pk2(h[0], h[1]), pk2(h[2], h[3]), pk2(h[4], h[5]), pk2(h[6], h[7])};
  if (tid < 30) *(u32x4*)(smem + K2_U + 16 * tid) = u32x4{0, 0, 0, 0};
  STAMP(2);
  __syncthreads();
  STAMP(3);
  __builtin_amdgcn_sched_barrier(0);
  const int r = lane & 15, g = lane >> 4;
  const int sg = (3 - r) & 3;
  const unsigned abase = sg * CP_STRIDE + 2 * (2031 + 8 * g - r - sg);
  const unsigned bbase = K2_U + 2 * (240 + 16 * r - 16 + 8 * g);
  f32x4 acc[8] = {};
#pragma unroll
  for (int cc = 0; cc < 2; ++cc) {
    const int c0 = 2 * wv + cc;
    bf16x8 A[8], B[8];
#pragma unroll
    for (int i = 0; i < 8; ++i) {
      const char* pa = smem + abase - 64 * c0 - 512 * i;
      u32x2 lo = *(const u32x2*)pa, hi = *(const u32x2*)(pa + 8);
      A[i] = __builtin_bit_cast(bf16x8, u32x4{lo.x, lo.y, hi.x, hi.y});
      B[i] = *(const bf16x8*)(smem + bbase - 64 * c0 + 512 * i);
    }
#pragma unroll
    for (int c1 = 0; c1 < 8; ++c1)
#pragma unroll
      for (int i = c1; i < 8; ++i) acc[i] = __builtin_amdgcn_mfma_f32_16x16x32_bf16(A[c1], B[i - c1], acc[i], 0, 0, 0);
  }
  float* red = (float*)(smem + K2_RED);
  STAMP(4);
  __syncthreads();
  STAMP(5);
#pragma unroll
  for (int i = 0; i < 8; ++i) *(f32x4*)(red + wv * 2560 + (16 * i + r) * 20 + 4 * g) = acc[i];
  __syncthreads();
  STAMP(6);
  if (tid == 0) {
    u16* mf = (u16*)(ws + WS_MODF);
    mf[d] = (u16)pk2((mv[3][0] + mv[3][1]) + (mv[3][2] + mv[3][3]), 0.f);
    mf[D + d] = (u16)pk2(1.f + rb((mv[4][0] + mv[4][1]) + (mv[4][2] + mv[4][3])), 0.f);
    mf[2 * D + d] = (u16)pk2((mv[5][0] + mv[5][1]) + (mv[5][2] + mv[5][3]), 0.f);
  }
  float y[8];
  {
    const int o = (tid >> 1) * 20 + (tid & 1) * 8;
#pragma unroll
    for (int hh = 0; hh < 2; ++hh) {
      f32x4 a = *(const f32x4*)(red + o + 4 * hh), b = *(const f32x4*)(red + 2560 + o + 4 * hh);
      f32x4 c = *(const f32x4*)(red + 5120 + o + 4 * hh), e = *(const f32x4*)(red + 7680 + o + 4 * hh);
      f32x4 s = (a + b) + (c + e);
#pragma unroll
      for (int k = 0; k < 4; ++k) y[4 * hh + k] = s[k];
    }
  }
  float o[8];
#pragma unroll
  for (int e = 0; e < 8; ++e) { float y2 = rb(rb(y[e]) + rb(h[e] * dsk)); o[e] = elem(xv, e) + rb(ga * y2); }
  *(u32x4*)((u16*)(ws + WS_X1T) + (size_t)d * L + 8 * tid) = u32x4{pk2(o[0], o[1]), pk2(o[2], o[3]), pk2(o[4], o[5]), pk2(o[6], o[7])};
  STAMP(7);
  STAMP(8);
  KT_END(2, ws);
}

constexpr int K3_H = 73728, K3_ST = 2 * 73728, K3_LDS = 2 * 73728 + 8192;
#define LDIDX(i) (i)
constexpr int RING = 16;
DEV unsigned swiglu_tile(const f32x4& c) {
  auto s0 = __builtin_amdgcn_permlane32_swap(__float_as_uint(c[0]), __float_as_uint(c[2]), false, false);
  auto s1 = __builtin_amdgcn_permlane32_swap(__float_as_uint(c[1]), __float_as_uint(c[3]), false, false);
  const f32x2 gt = {__uint_as_float(s0[0]), __uint_as_float(s1[0])}, up = {__uint_as_float(s0[1]), __uint_as_float(s1[1])};
  const f32x2 ar = gt * -1.44269504088896341f;
  const f32x2 dn = f32x2{__builtin_amdgcn_exp2f(ar.x), __builtin_amdgcn_exp2f(ar.y)} + 1.f;
  const f32x2 hv = gt * f32x2{__builtin_amdgcn_rcpf(dn.x), __builtin_amdgcn_rcpf(dn.y)} * up;
  return pk2(hv.x, hv.y);
}
DEV bf16x8 tfrag(const u32x4 (&raw)[8], int i) {
  const unsigned sel = (i & 1) ? 0x07060302u : 0x05040100u;
  u32x4 o;
#pragma unroll
  for (int q = 0; q < 4; ++q) o[q] = __builtin_amdgcn_perm(raw[2 * q + 1][i >> 1], raw[2 * q][i >> 1], sel);
  return __builtin_bit_cast(bf16x8, o);
}
template <int NT>
DEV void ffn_phase2(__amdgpu_buffer_rsrc_t rs, int wv, int lane, char* smem, char* pout, bf16x8 (&ring)[RING], char* ws STAMP_ARG) {
  const unsigned vo = lane * 16;
  constexpr int F0 = NT == 5 ? 36 : 0, NFR = F0 + 18 * NT;
  auto ldw = [&](int idx) { return __builtin_bit_cast(bf16x8, __builtin_amdgcn_raw_buffer_load_b128(rs, vo, idx * 1024, 0)); };
  bf16x8 b[2][4];
  f32x4 oc[NT][4];
#pragma unroll
  for (int tt = 0; tt < 4; ++tt) b[0][tt] = *(const bf16x8*)(smem + K3_H + tt * 1024 + lane * 16);
#pragma unroll
  for (int ks = 0; ks < 18; ++ks) {
    if (ks < 17) {
#pragma unroll
      for (int tt = 0; tt < 4; ++tt) b[(ks + 1) & 1][tt] = *(const bf16x8*)(smem + K3_H + (ks + 1) * 4096 + tt * 1024 + lane * 16);
    }
#pragma unroll
    for (int ti = 0; ti < NT; ++ti) {
      const int i = F0 + ks * NT + ti;
#pragma unroll
      for (int tt = 0; tt < 4; ++tt)
        oc[ti][tt] = __builtin_amdgcn_mfma_f32_16x16x32_bf16(ring[i % RING], b[ks & 1][tt], ks == 0 ? f32x4{0.f, 0.f, 0.f, 0.f} : oc[ti][tt], 0, 0, 0);
      if (i + RING < NFR) ring[i % RING] = ldw(i + RING);
      __builtin_amdgcn_sched_barrier(0);
    }
  }
  STAMP(8);
  const int dt0 = NT == 5 ? 5 * wv : 20 + 4 * (wv - 4);
#pragma unroll
  for (int ti = 0; ti < NT; ++ti)
#pragma unroll
    for (int tp = 0; tp < 2; ++tp) {
      const f32x4 u = oc[ti][2 * tp], v = oc[ti][2 * tp + 1];
      *(u32x4*)(pout + (((dt0 + ti) * 2 + tp) * 64 + lane) * 16) = u32x4{pk2(u[0], u[1]), pk2(u[2], u[3]), pk2(v[0], v[1]), pk2(v[2], v[3])};
    }
  STAMP(9);
  STAMP_OUT(ws, 10);
}

DEV void ffn_phase1(const char* wp, const char* whead, __amdgpu_buffer_rsrc_t rs, int wv, int lane, char* smem, u32x4 (&raw)[2][8], char* pout, char* ws STAMP_ARG) {
  bf16x8 ring[RING];
  const unsigned vo = lane * 16;
  auto ldw = [&](int idx) { return __builtin_bit_cast(bf16x8, __builtin_amdgcn_raw_buffer_load_b128(rs, vo, idx * 1024, 0)); };
  unsigned boff[4];
#pragma unroll
  for (int tt = 0; tt < 4; ++tt) boff[tt] = tt * 1024 + ((lane ^ ((2 * tt + ((lane >> 3) & 1)) & 7)) * 16);
  f32x4 acc[8][4] = {};
  bf16x8 b[4];
#pragma unroll
  for (int tt = 0; tt < 4; ++tt) b[tt] = *(const bf16x8*)(smem + boff[tt]);
  bf16x8 a = tfrag(raw[0], 0);
  auto ldrow = [&](int j) {
    const char* p;
    asm("v_lshl_add_u64 %0, %1, 0, %2" : "=v"(p) : "v"(wp), "s"((unsigned long long)j * (F * 2)));
    return *(const __attribute__((address_space(1))) u32x4*)p;
  };
#define SGB(m, n) __builtin_amdgcn_sched_group_barrier(m, n, 0)
  auto step = [&](int par, int ksn, unsigned inc) {
    bf16x8 hi[4];
#pragma unroll
    for (int i = 0; i < 4; ++i) {
      bf16x8 an;
      if (i < 3) an = tfrag(raw[par], i + 1);
      hi[i] = tfrag(raw[par], 4 + i);
#pragma unroll
      for (int tt = 0; tt < 4; ++tt) acc[i][tt] = __builtin_amdgcn_mfma_f32_16x16x32_bf16(a, b[tt], acc[i][tt], 0, 0, 0);
      if (i < 3) a = an;
      SGB(0x8, 1); SGB(0x2, 2); SGB(0x8, 1); SGB(0x2, 2); SGB(0x8, 1); SGB(0x2, 2); SGB(0x8, 1); SGB(0x2, 2);
      __builtin_amdgcn_sched_barrier(0);
    }
#pragma unroll
    for (int i = 4; i < 6; ++i) {
#pragma unroll
      for (int j = 0; j < 3; ++j) raw[par][3 * (i - 4) + j] = ldrow(3 * (i - 4) + j);
#pragma unroll
      for (int tt = 0; tt < 4; ++tt) acc[i][tt] = __builtin_amdgcn_mfma_f32_16x16x32_bf16(hi[i - 4], b[tt], acc[i][tt], 0, 0, 0);
      SGB(0x8, 1); SGB(0x2, 1); SGB(0x20, 1); SGB(0x8, 1); SGB(0x2, 1); SGB(0x20, 1); SGB(0x8, 1); SGB(0x2, 1); SGB(0x20, 1); SGB(0x8, 1);
      __builtin_amdgcn_sched_barrier(0);
    }
    const char* bn = smem + ksn * 4096;
#pragma unroll
    for (int tt = 0; tt < 4; ++tt) {
      if (tt < 2) raw[par][6 + tt] = ldrow(6 + tt);
      if (tt == 2) { a = tfrag(raw[par ^ 1], 0); wp += inc; }
      acc[6][tt] = __builtin_amdgcn_mfma_f32_16x16x32_bf16(hi[2], b[tt], acc[6][tt], 0, 0, 0);
      acc[7][tt] = __builtin_amdgcn_mfma_f32_16x16x32_bf16(hi[3], b[tt], acc[7][tt], 0, 0, 0);
      b[tt] = *(const bf16x8*)(bn + boff[tt]);
      SGB(0x8, 1); SGB(0x2, 3); SGB(0x20, 1); SGB(0x8, 1); SGB(0x2, 3); SGB(0x100, 1);
      __builtin_amdgcn_sched_barrier(0);
    }
  };
#pragma unroll 1
  for (int it = 0; it < 9; ++it) {
    const bool last = it == 8;
    if (last) wp = whead;
    const unsigned inc = (last ? 8 : 32) * (F * 2);
    step(0, 2 * it + 1, inc);
    step(1, last ? 0 : 2 * it + 2, inc);
  }
#pragma unroll
  for (int i = 0; i < 16; ++i) ring[i] = __builtin_bit_cast(bf16x8, raw[i >> 3][i & 7]);
  STAMP(5);
  char* hw0 = smem + K3_H + (2 * wv) * 4096 + lane * 16;
  auto put4 = [&](int h, int tt) {
    *(u32x4*)(hw0 + h * 4096 + tt * 1024) = u32x4{swiglu_tile(acc[4 * h][tt]), swiglu_tile(acc[4 * h + 1][tt]), swiglu_tile(acc[4 * h + 2][tt]), swiglu_tile(acc[4 * h + 3][tt])};
  };
  if (wv < 4) {
    put4(0, 0); put4(0, 1);
    __builtin_amdgcn_sched_barrier(0);
    f32x4 am[2][4] = {};
#pragma unroll
    for (int ks = 0; ks < 18; ++ks) {
#pragma unroll
      for (int tt = 0; tt < 4; ++tt) {
        am[0][tt] = __builtin_amdgcn_mfma_f32_16x16x32_bf16(ring[(2 * ks) % RING], b[tt], am[0][tt], 0, 0, 0);
        am[1][tt] = __builtin_amdgcn_mfma_f32_16x16x32_bf16(ring[(2 * ks + 1) % RING], b[tt], am[1][tt], 0, 0, 0);
        if (ks < 17) b[tt] = *(const bf16x8*)(smem + (ks + 1) * 4096 + boff[tt]);
      }
      ring[(2 * ks) % RING] = ldw(2 * ks + RING);
      ring[(2 * ks + 1) % RING] = ldw(2 * ks + 1 + RING);
      if (ks < 18 && ks % 3 == 0) put4((ks / 3 + 2) >> 2, (ks / 3 + 2) & 3);
      __builtin_amdgcn_sched_barrier(0);
    }
    char* hw8 = smem + K3_H + (16 + (wv >> 1)) * 4096 + lane * 16 + (wv & 1) * 8;
#pragma unroll
    for (int tt = 0; tt < 4; ++tt) *(u32x2*)(hw8 + tt * 1024) = u32x2{swiglu_tile(am[0][tt]), swiglu_tile(am[1][tt])};
    STAMP(6);
    __syncthreads();
    STAMP(7);
    ffn_phase2<5>(rs, wv, lane, smem, pout, ring, ws STAMP_PASS);
    __builtin_amdgcn_endpgm();
  }
#pragma unroll
  for (int q = 0; q < 8; ++q) put4(q >> 2, q & 3);
  STAMP(6);
  __syncthreads();
  STAMP(7);
  ffn_phase2<4>(rs, wv, lane, smem, pout, ring, ws STAMP_PASS);
}
__global__ __launch_bounds__(512, 2) void k3_ffn(const u16* w1, const u16* w3, char* ws) {
  extern __shared__ __attribute__((aligned(16))) char smem[];
  const int tid = threadIdx.x, lane = tid & 63;
  const int wv = __builtin_amdgcn_readfirstlane(tid >> 6);
  STAMP_DECL
  STAMP(0);
  const int xc = blockIdx.x & 7, bi = blockIdx.x >> 3;
  const int s = (xc % (8 / SPX)) * SPX + bi % SPX, mt = (xc / (8 / SPX)) * (32 / SPX) + bi / SPX;
  const u16* x1T = (const u16*)(ws + WS_X1T) + 64 * mt;
  const u16* mf = (const u16*)(ws + WS_MODF);
  const int tc = tid & 7, cg = tid >> 3;
  u32x4 r[9];
#pragma unroll
  for (int e = 0; e < 8; ++e) r[e] = *(const u32x4*)(x1T + (size_t)(8 * cg + e) * L + 8 * tc);
  r[8] = *(const u32x4*)(x1T + (size_t)(512 + cg) * L + 8 * tc);
  const u32x4 sbv = *(const u32x4*)(mf + 8 * cg), cbv = *(const u32x4*)(mf + D + 8 * cg);
  const float sb8 = bf2f(mf[512 + cg]), cb8 = bf2f(mf[D + 512 + cg]);
  __builtin_amdgcn_sched_barrier(0);
  asm volatile("s_waitcnt vmcnt(0)" ::: "memory");
  STAMP(1);
  __builtin_amdgcn_sched_barrier(0);
  f32x2 xv[9][4];
#pragma unroll
  for (int e = 0; e < 9; ++e)
#pragma unroll
    for (int t = 0; t < 4; ++t) xv[e][t] = f32x2{bflo(r[e][t]), bfhi(r[e][t])};
  float s1, s2;
  {
    f32x2 p1[4], p2[4];
#pragma unroll
    for (int t = 0; t < 4; ++t) {
      p1[t] = xv[0][t]; p2[t] = xv[0][t] * xv[0][t];
#pragma unroll
      for (int e = 1; e < 9; ++e) { p1[t] += xv[e][t]; p2[t] += xv[e][t] * xv[e][t]; }
    }
    auto sw32 = [](float a, float b) { auto q = __builtin_amdgcn_permlane32_swap(__float_as_uint(a), __float_as_uint(b), false, false); return __uint_as_float(q[0]) + __uint_as_float(q[1]); };
    auto sw16 = [](float a, float b) { auto q = __builtin_amdgcn_permlane16_swap(__float_as_uint(a), __float_as_uint(b), false, false); return __uint_as_float(q[0]) + __uint_as_float(q[1]); };
    float u1[4], u2[4];
#pragma unroll
    for (int t = 0; t < 4; ++t) { u1[t] = sw32(p1[t >> 1][t & 1], p1[2 + (t >> 1)][t & 1]); u2[t] = sw32(p2[t >> 1][t & 1], p2[2 + (t >> 1)][t & 1]); }
    const float v10 = sw16(u1[0], u1[2]), v11 = sw16(u1[1], u1[3]), v20 = sw16(u2[0], u2[2]), v21 = sw16(u2[1], u2[3]);
    const bool b3 = lane & 8;
    auto ror8 = [](float v) { return __int_as_float(__builtin_amdgcn_update_dpp(0, __float_as_int(v), 0x128, 0xf, 0xf, false)); };
    s1 = (b3 ? v11 : v10) + ror8(b3 ? v10 : v11);
    s2 = (b3 ? v21 : v20) + ror8(b3 ? v20 : v21);
  }
  float* st = (float*)(smem + K3_ST);
  *(f32x2*)(st + (wv * 64 + 8 * tc + (lane >> 3)) * 2) = f32x2{s1, s2};
  __builtin_amdgcn_sched_barrier(0);
  char* wbase = ws + WS_WT + ((size_t)s * FR_S + wave_frag_off(wv)) * 1024;
  __amdgpu_buffer_rsrc_t rs = __builtin_amdgcn_make_buffer_rsrc(wbase, 0, 126 * 1024, 0x00020000);
  const char* wp = (const char*)((lane & 8) ? w3 : w1) + ((size_t)(8 * (lane >> 4)) * F + 576 * s + 64 * wv + 8 * (lane & 7)) * 2;
  u32x4 raw[2][8];
#pragma unroll
  for (int i = 0; i < 2; ++i) {
#pragma unroll
    for (int j = 0; j < 8; ++j) raw[i][j] = *(const u32x4*)(wp + (size_t)j * (F * 2));
    wp += (size_t)32 * (F * 2);
  }
  __builtin_amdgcn_sched_barrier(0);
  STAMP(2);
  asm volatile("s_waitcnt lgkmcnt(0)" ::: "memory");
  __builtin_amdgcn_s_barrier();
  STAMP(3);
  float* sw = (float*)(smem + K3_ST + 4096) + wv * 128;
  {
    float a = 0.f, b = 0.f;
#pragma unroll
    for (int w = 0; w < 8; ++w) { f32x2 v = *(const f32x2*)(st + (w * 64 + lane) * 2); a += v.x; b += v.y; }
    const float mean = a * (1.f / D);
    const float var = fmaxf(b * (1.f / D) - mean * mean, 0.f);
    const float rd = 1.f / rb(sqrtf(rb(rb(var) + 1e-5f)));
    sw[lane] = rd; sw[64 + lane] = -rb(mean) * rd;
  }
  const f32x4 rd0 = *(const f32x4*)(sw + 8 * tc), rd1 = *(const f32x4*)(sw + 8 * tc + 4);
  const f32x4 nm0 = *(const f32x4*)(sw + 64 + 8 * tc), nm1 = *(const f32x4*)(sw + 64 + 8 * tc + 4);
  const f32x2 rdp[4] = {{rd0[0], rd0[1]}, {rd0[2], rd0[3]}, {rd1[0], rd1[1]}, {rd1[2], rd1[3]}};
  const f32x2 nmp[4] = {{nm0[0], nm0[1]}, {nm0[2], nm0[3]}, {nm1[0], nm1[1]}, {nm1[2], nm1[3]}};
  char* base = smem + (cg >> 2) * 4096 + (tc >> 1) * 1024 + (cg & 3) * 256 + (tc & 1) * 128;
  char* base8 = smem + (16 + (wv >> 2)) * 4096 + (tc >> 1) * 1024 + (wv & 3) * 256 + (tc & 1) * 128 + (cg & 7) * 2;
#pragma unroll
  for (int t = 0; t < 4; ++t) {
    f32x2 hv[9];
#pragma unroll
    for (int e = 0; e < 9; ++e) {
      const float cb = e < 8 ? elem(cbv, e) : cb8, sb = e < 8 ? elem(sbv, e) : sb8;
      hv[e] = __builtin_elementwise_fma(__builtin_elementwise_fma(xv[e][t], rdp[t], nmp[t]), f32x2{cb, cb}, f32x2{sb, sb});
    }
#pragma unroll
    for (int p = 0; p < 2; ++p) {
      *(u32x4*)(base + (((2 * t + p) ^ tc) * 16)) = u32x4{pk2(hv[0][p], hv[1][p]), pk2(hv[2][p], hv[3][p]), pk2(hv[4][p], hv[5][p]), pk2(hv[6][p], hv[7][p])};
      *(u16*)(base8 + (((2 * t + p) ^ tc) * 16)) = (u16)pk2(hv[8][p], 0.f);
    }
  }
  char* pout = ws + WS_P + ((size_t)mt * 8 + s) * 73728;
  asm volatile("s_waitcnt lgkmcnt(0)" ::: "memory");
  __builtin_amdgcn_s_barrier();
  STAMP(4);
  ffn_phase1(wp, ws + WS_WT + ((size_t)s * FR_S + wv) * 1024 + lane * 16, rs, wv, lane, smem, raw, pout, ws STAMP_PASS);
}

__global__ __launch_bounds__(256) void k4_out(const char* ws, u16* out) {
  KT_BEGIN(4)
  const int gid = blockIdx.x * 256 + threadIdx.x;
  const int mt = gid / 4608, rem = gid % 4608, lane = rem & 63, tp = (rem >> 6) & 1, dt = rem >> 7;
  const char* p = ws + WS_P + (size_t)mt * 8 * 73728 + (size_t)rem * 16;
  u32x4 v[8];
#pragma unroll
  for (int s = 0; s < 8; ++s) v[s] = *(const u32x4*)(p + (size_t)s * 73728);
  const int d0 = 16 * dt + 4 * (lane >> 4), t0 = 64 * mt + 32 * tp + (lane & 15);
  const u16* x1T = (const u16*)(ws + WS_X1T);
  const u16* mf = (const u16*)(ws + WS_MODF);
  float xr[8], gb[4];
#pragma unroll
  for (int e = 0; e < 4; ++e) { xr[e] = bf2f(x1T[(size_t)(d0 + e) * L + t0]); xr[4 + e] = bf2f(x1T[(size_t)(d0 + e) * L + t0 + 16]); gb[e] = bf2f(mf[2 * D + d0 + e]); }
  float o[8];
#pragma unroll
  for (int e = 0; e < 8; ++e) {
    float a = ((elem(v[0], e) + elem(v[1], e)) + (elem(v[2], e) + elem(v[3], e))) + ((elem(v[4], e) + elem(v[5], e)) + (elem(v[6], e) + elem(v[7], e)));
    o[e] = xr[e] + rb(gb[e & 3] * rb(a));
  }
  *(u32x2*)(out + (size_t)t0 * D + d0) = u32x2{pk2(o[0], o[1]), pk2(o[2], o[3])};
  *(u32x2*)(out + (size_t)(t0 + 16) * D + d0) = u32x2{pk2(o[4], o[5]), pk2(o[6], o[7])};
  KT_END(4, ws);
}

extern "C" void kernel_launch(void* const* d_in, const int* in_sizes, int n_in, void* d_out, int out_size, void* d_ws,
                              size_t ws_size, hipStream_t stream) {
  const u16* x = (const u16*)d_in[0];
  const u16 *Are = (const u16*)d_in[1], *Aim = (const u16*)d_in[2], *Bre = (const u16*)d_in[3], *Bim = (const u16*)d_in[4];
  const u16 *Cre = (const u16*)d_in[5], *Cim = (const u16*)d_in[6], *logdt = (const u16*)d_in[7], *Dskip = (const u16*)d_in[8];
  const u16 *w1 = (const u16*)d_in[10], *w3 = (const u16*)d_in[11], *w2 = (const u16*)d_in[12];
  const u16 *c = (const u16*)d_in[13], *wmod = (const u16*)d_in[14];
  char* ws = (char*)d_ws;
  (void)hipFuncSetAttribute((const void*)k3_ffn, hipFuncAttributeMaxDynamicSharedMemorySize, K3_LDS);
  (void)hipFuncSetAttribute((const void*)k1_prep, hipFuncAttributeMaxDynamicSharedMemorySize, K1_LDS);
  k1_prep<<<256, 1024, K1_LDS, stream>>>(x, Are, Aim, Bre, Bim, Cre, Cim, logdt, w1, w3, w2, c, wmod, ws);
  k2_conv<<<D, 256, K2_LDS, stream>>>(Dskip, w1, w3, w2, ws);
  k3_ffn<<<256, 512, K3_LDS, stream>>>(w1, w3, ws);
  k4_out<<<576, 256, 0, stream>>>(ws, (u16*)d_out);
}
```

```cpp
#include <hip/hip_runtime.h>
#include <hip/hip_bf16.h>
#include <stdint.h>

typedef __attribute__((ext_vector_type(8))) __bf16 bf16x8;
typedef __attribute__((ext_vector_type(2))) __bf16 bf16x2;
typedef __attribute__((ext_vector_type(8))) _Float16 f16x8;
typedef __attribute__((ext_vector_type(4))) float f32x4;
typedef __attribute__((ext_vector_type(2))) float f32x2;
typedef __attribute__((ext_vector_type(4))) unsigned u32x4;
typedef __attribute__((ext_vector_type(2))) unsigned u32x2;
typedef __attribute__((ext_vector_type(4))) short s16x4;
typedef unsigned short u16;
#define LDSP __attribute__((address_space(3)))
#define DEV __device__ __forceinline__

#define STAMP(i)
#define STAMP_DECL
#define STAMP_ARG
#define STAMP_PASS
#define STAMP_OUT(ws, n)
#define KT_BEGIN(k)
#define KT_END(k, ws)
#define K2PRIO 1
#define PREF1 0
#define LBAR 0
#define PREF 1
#define PREF2 0
#define SPX 2
constexpr int L = 2048, D = 576, NM = 128, F = 4608;
constexpr int KR_STRIDE = 2064;
constexpr int MODK = 4;
constexpr int FR_S = 144 + 664;

constexpr size_t WS_MODP = 0;
constexpr size_t WS_MODF = 57344;
constexpr size_t WS_CNT  = 61440;
constexpr size_t WS_XT   = 65536;
constexpr size_t WS_ZT   = WS_XT + (size_t)D * L * 2;
constexpr size_t WS_X1T  = WS_ZT + (size_t)D * L * 2;
constexpr size_t WS_KR   = WS_X1T + (size_t)D * L * 2;
constexpr size_t WS_WT   = (WS_KR + (size_t)D * KR_STRIDE * 2 + 4095) / 4096 * 4096;
constexpr size_t WS_P    = WS_WT + (size_t)8 * FR_S * 1024;

DEV float bf2f(unsigned v) { return __uint_as_float(v << 16); }
DEV float bflo(unsigned v) { return __uint_as_float(v << 16); }
DEV float bfhi(unsigned v) { return __uint_as_float(v & 0xffff0000u); }
DEV float rb(float x) { return (float)(__bf16)x; }
DEV unsigned pk2(float a, float b) { f32x2 v = {a, b}; bf16x2 r = __builtin_convertvector(v, bf16x2); return __builtin_bit_cast(unsigned, r); }
DEV float elem(const u32x4& v, int i) { unsigned w = v[i >> 1]; return (i & 1) ? bfhi(w) : bflo(w); }
DEV int wave_frag_off(int w) { return 144 - 16 + (w < 4 ? 110 * w : 440 + 56 * (w - 4)); }
DEV int frag_pos(int s, int w, int i) { return s * FR_S + (i < 16 ? i * 9 + w : wave_frag_off(w) + i); }

DEV void lds_barrier() { asm volatile("s_waitcnt lgkmcnt(0)" ::: "memory"); __builtin_amdgcn_s_barrier(); asm volatile("" ::: "memory"); }
DEV void retile1_load(int u, const u16* w1, const u16* w3, u32x4 (&r)[3]) {
  const int tid = threadIdx.x & 255, s = u & 7, ks = u >> 3;
  const size_t o = (size_t)(32 * ks + (tid >> 3)) * F + 576 * s + 512 + (tid & 7) * 8;
  r[0] = *(const u32x4*)(w1 + o);
  r[1] = *(const u32x4*)(w3 + o);
}
DEV void retile2_load(int u, const u16* w2, u32x4 (&r)[3]) {
  const int tid = threadIdx.x & 255, s = u & 7, v = (u >> 3) - 18, ks = (v * 43) >> 7, th = v - 3 * ks;
  const int kk = tid >> 3, gg = kk >> 3, jj = kk & 7, cp = 4 * (gg & 1) + 2 * (gg >> 1) + (jj & 1);
  const bool nrm = ks < 16;
  const int f = (nrm ? 64 * (ks >> 1) + 4 * (ks & 1) : 32 * ks) + (nrm ? 8 * cp + (jj >> 1) : cp + 8 * (jj >> 1));
  const u16* p = w2 + (size_t)(576 * s + f) * D + 192 * th + (tid & 7) * 8;
#pragma unroll
  for (int i = 0; i < 3; ++i) r[i] = *(const u32x4*)(p + 64 * i);
}
template <int N> DEV void retile_stage(const u32x4 (&r)[3], char* st) {
  const int tid = threadIdx.x & 255;
  if (N == 2) { *(u32x4*)(st + tid * 16) = r[0]; *(u32x4*)(st + 4096 + tid * 16) = r[1]; }
  else {
#pragma unroll
    for (int i = 0; i < 3; ++i) *(u32x4*)(st + (tid >> 3) * 384 + (tid & 7) * 16 + 128 * i) = r[i];
  }
}
template <int RS> DEV u32x4 tr_frag(const char* p) {
  s16x4 lo = __builtin_amdgcn_ds_read_tr16_b64_v4i16((LDSP s16x4*)p);
  s16x4 hi = __builtin_amdgcn_ds_read_tr16_b64_v4i16((LDSP s16x4*)(p + 4 * RS));
  u32x2 l2 = __builtin_bit_cast(u32x2, lo), h2 = __builtin_bit_cast(u32x2, hi);
  return u32x4{l2.x, l2.y, h2.x, h2.y};
}
DEV void retile1_emit(int u, const char* st, char* wt) {
  const int lane = threadIdx.x & 63, wv = __builtin_amdgcn_readfirstlane((threadIdx.x >> 6) & 3);
  const int s = u & 7, ks = u >> 3;
  const int g = lane >> 4, qq = (lane & 15) >> 2, pp = lane & 3;
  const char* p = st + (pp >> 1) * 4096 + (8 * g + qq) * 128 + (pp & 1) * 8 + wv * 32;
#pragma unroll
  for (int i = 0; i < 2; ++i)
    *(u32x4*)(wt + (size_t)frag_pos(s, wv, 2 * ks + i) * 1024 + lane * 16) = tr_frag<128>(p + i * 16);
}
DEV void retile2_emit(int u, const char* st, char* wt) {
  const int lane = threadIdx.x & 63, wv = __builtin_amdgcn_readfirstlane((threadIdx.x >> 6) & 3);
  const int s = u & 7, v = (u >> 3) - 18, ks = (v * 43) >> 7, th = v - 3 * ks;
  const int g = lane >> 4, qq = (lane & 15) >> 2, pp = lane & 3;
  const char* p = st + (8 * g + qq) * 384 + pp * 8 + wv * 96;
#pragma unroll
  for (int i = 0; i < 3; ++i) {
    const int dt = 12 * th + wv * 3 + i;
    const int wa = (dt * 13) >> 6, pa = 128 + 110 * wa + 36 + ks * 5 + (dt - 5 * wa);
    const int wb = (dt - 20) >> 2, ib = ks * 4 + ((dt - 20) & 3);
    const int pb = ib < 16 ? ib * 9 + 4 + wb : 128 + 440 + 56 * wb + ib;
    const int pos = __builtin_amdgcn_readfirstlane(s * FR_S + (dt < 20 ? pa : pb));
    *(u32x4*)(wt + (size_t)pos * 1024 + lane * 16) = tr_frag<384>(p + i * 32);
  }
}

#define RT_K1 144
struct cf { float re, im; };
DEV cf cmul(cf a, cf b) { return cf{a.re * b.re - a.im * b.im, a.re * b.im + a.im * b.re}; }
constexpr int LN_RS = 1152 + 16;
DEV void ln1_block(int u, const u16* x, u16* xT, float* stat, char* smem) {
  const int tid = threadIdx.x & 255, tok = tid >> 4, c16 = tid & 15, lane = tid & 63, wv = __builtin_amdgcn_readfirstlane(tid >> 6);
  const int t0 = 16 * ((u & 7) * 16 + (u >> 3));
  const u16* xr = x + (size_t)(t0 + tok) * D + c16 * 8;
  u32x4 r[5];
#pragma unroll
  for (int i = 0; i < 4; ++i) r[i] = *(const u32x4*)(xr + 128 * i);
  r[4] = c16 < 8 ? *(const u32x4*)(xr + 512) : u32x4{0, 0, 0, 0};
#pragma unroll
  for (int i = 0; i < 4; ++i) *(u32x4*)(smem + tok * LN_RS + (c16 + 16 * i) * 16) = r[i];
  if (c16 < 8) *(u32x4*)(smem + tok * LN_RS + (c16 + 64) * 16) = r[4];
  f32x2 sp = {0.f, 0.f};
#pragma unroll
  for (int i = 0; i < 5; ++i)
#pragma unroll
    for (int e = 0; e < 4; ++e) sp += f32x2{bflo(r[i][e]), bfhi(r[i][e])};
  auto sum16 = [](float v) {
    v += __int_as_float(__builtin_amdgcn_update_dpp(0, __float_as_int(v), 0xB1, 0xf, 0xf, false));
    v += __int_as_float(__builtin_amdgcn_update_dpp(0, __float_as_int(v), 0x4E, 0xf, 0xf, false));
    v += __int_as_float(__builtin_amdgcn_update_dpp(0, __float_as_int(v), 0x124, 0xf, 0xf, false));
    v += __int_as_float(__builtin_amdgcn_update_dpp(0, __float_as_int(v), 0x128, 0xf, 0xf, false));
    return v;
  };
  const float mean = sum16(sp.x + sp.y) * (1.f / D);
  f32x2 qp = {0.f, 0.f};
#pragma unroll
  for (int i = 0; i < 5; ++i)
#pragma unroll
    for (int e = 0; e < 4; ++e) {
      f32x2 dl = f32x2{bflo(r[i][e]), bfhi(r[i][e])} - mean;
      if (i == 4 && c16 >= 8) dl = f32x2{0.f, 0.f};
      qp += dl * dl;
    }
  const float q = sum16(qp.x + qp.y);
  if (c16 == 0) { stat[t0 + tok] = rb(mean); stat[L + t0 + tok] = 1.f / rb(sqrtf(rb(rb(q * (1.f / D)) + 1e-5f))); }
  __syncthreads();
  const int g = lane >> 4, qq = (lane & 15) >> 2, pp = lane & 3;
  const char* p = smem + (8 * (g & 1) + qq) * LN_RS + pp * 8 + (g >> 1) * 32 + wv * 64;
  u16* o = xT + (size_t)(32 * wv + 16 * (g >> 1) + (lane & 15)) * L + t0 + 8 * (g & 1);
#pragma unroll
  for (int i = 0; i < 5; ++i)
    if (i < 4 || wv < 2) *(u32x4*)(o + (size_t)i * 128 * L) = tr_frag<LN_RS>(p + i * 256);
}

DEV void mod_block(int b, const u16* c, const u16* wmod, float* modp, char* smem) {
  const int tid = threadIdx.x & 255, c16 = tid & 15, rg = tid >> 4, lane = tid & 63, wv = tid >> 6;
  const int cg = b % 27, kq = b / 27;
  u32x4 r[9]; unsigned cv[9];
#pragma unroll
  for (int i = 0; i < 9; ++i) {
    const int k = 144 * kq + rg + 16 * i;
    r[i] = *(const u32x4*)(wmod + (size_t)k * 3456 + 128 * cg + 8 * c16);
    cv[i] = c[k];
  }
  float acc[8] = {};
#pragma unroll
  for (int i = 0; i < 9; ++i) {
    const float x = bf2f(cv[i]);
    const float sv = rb(x / (1.f + __expf(-x)));
#pragma unroll
    for (int e = 0; e < 8; ++e) acc[e] += sv * elem(r[i], e);
  }
#pragma unroll
  for (int e = 0; e < 8; ++e) { acc[e] += __shfl_xor(acc[e], 16); acc[e] += __shfl_xor(acc[e], 32); }
  float* red = (float*)smem;
  if (lane < 16)
#pragma unroll
    for (int e = 0; e < 8; ++e) red[wv * 128 + lane * 8 + e] = acc[e];
  __syncthreads();
  if (tid < 128) modp[kq * 3456 + 128 * cg + tid] = (red[tid] + red[128 + tid]) + (red[256 + tid] + red[384 + tid]);
}

constexpr int RT0 = 272;
constexpr int K0_LDS = 16 * LN_RS;
__global__ __launch_bounds__(256) void k0_prep(const u16* x, const u16* w1, const u16* w3, const u16* w2, const u16* c,
                                               const u16* wmod, char* ws) {
  extern __shared__ __attribute__((aligned(16))) char smem[];
  KT_BEGIN(1)
  const int b = blockIdx.x;
  if (b < 128) {
    ln1_block(b, x, (u16*)(ws + WS_XT), (float*)(ws + WS_ZT), smem);
  } else if (b < 240) {
    if (b < 128 + 27 * MODK) mod_block(b - 128, c, wmod, (float*)(ws + WS_MODP), smem);
    else if (b == 239) ((unsigned*)(ws + WS_CNT))[threadIdx.x * 4 + 0] = 0, ((unsigned*)(ws + WS_CNT))[threadIdx.x * 4 + 1] = 0,
                       ((unsigned*)(ws + WS_CNT))[threadIdx.x * 4 + 2] = 0, ((unsigned*)(ws + WS_CNT))[threadIdx.x * 4 + 3] = 0;
  } else {
    const int u = b - 240;
    u32x4 r0[3];
    if (u < 144) retile1_load(u, w1, w3, r0); else retile2_load(u, w2, r0);
    if (u < 144) retile_stage<2>(r0, smem); else retile_stage<3>(r0, smem);
    __syncthreads();
    if (u < 144) retile1_emit(u, smem, ws + WS_WT); else retile2_emit(u, smem, ws + WS_WT);
  }
  KT_END(1, ws);
}

constexpr int KG_RS = 528;
constexpr int CP_STRIDE = 4160;
constexpr int K1_U = 0;
constexpr int K1_TAB = 4608;
constexpr int K1_OUT = K1_TAB + 57 * KG_RS, K1_SC = K1_OUT + 660 * 4;
constexpr int K1_ST = 4 * 2560 * 4, K1_LDS = K1_ST + 12288;
__global__ __launch_bounds__(256, 3) void k1_conv(const u16* Are, const u16* Aim, const u16* Bre, const u16* Bim, const u16* Cre,
    const u16* Cim, const u16* logdt, const u16* Dskip, const u16* w1, const u16* w3, const u16* w2, char* ws) {
  extern __shared__ __attribute__((aligned(16))) char smem[];
  STAMP_DECL
  STAMP(0);
  const int d = blockIdx.x, tid = threadIdx.x, lane = tid & 63, wv = __builtin_amdgcn_readfirstlane(tid >> 6);
  const int p = lane & 15, g = lane >> 4;
  if (d < 256) __builtin_amdgcn_s_setprio(2); else if (d < 512) __builtin_amdgcn_s_setprio(1);
  float* tab = (float*)(smem + K1_OUT);
  float* scl = (float*)(smem + K1_SC);
  const int mode = tid & 127, n = d * NM + mode;
  const unsigned iare = Are[n], iaim = Aim[n], ibre = Bre[n], ibim = Bim[n], icre = Cre[n], icim = Cim[n], ildt = logdt[d];
  __builtin_amdgcn_sched_barrier(0);
  const u32x4 xv = *(const u32x4*)((const u16*)(ws + WS_XT) + (size_t)d * L + 8 * tid);
  const float* stat = (const float*)(ws + WS_ZT) + 8 * tid;
  const f32x4 m0 = *(const f32x4*)stat, m1 = *(const f32x4*)(stat + 4), r0 = *(const f32x4*)(stat + L), r1 = *(const f32x4*)(stat + L + 4);
  const __attribute__((address_space(4))) float* mp = (const __attribute__((address_space(4))) float*)(ws + WS_MODP) + d;
  float mv[6];
#pragma unroll
  for (int i = 0; i < 6; ++i) mv[i] = rb((mp[i * D] + mp[3456 + i * D]) + (mp[2 * 3456 + i * D] + mp[3 * 3456 + i * D]));
  const float dsk = bf2f(Dskip[d]);
  __builtin_amdgcn_sched_barrier(0);
  {
    const float dt = rb(expf(bf2f(ildt)));
    const float a2 = dt * bf2f(iare) * 1.44269504088896341f, br = dt * bf2f(iaim) * 0.15915494309189535f;
    cf cb = cmul(cf{bf2f(icre), bf2f(icim)}, cf{dt * bf2f(ibre), dt * bf2f(ibim)});
    STAMP(1);
    float mx = fmaxf(fabsf(cb.re), fabsf(cb.im));
    mx = fmaxf(mx, __int_as_float(__builtin_amdgcn_update_dpp(0, __float_as_int(mx), 0xB1, 0xf, 0xf, false)));
    mx = fmaxf(mx, __int_as_float(__builtin_amdgcn_update_dpp(0, __float_as_int(mx), 0x4E, 0xf, 0xf, false)));
    mx = fmaxf(mx, __int_as_float(__builtin_amdgcn_update_dpp(0, __float_as_int(mx), 0x124, 0xf, 0xf, false)));
    mx = fmaxf(mx, __int_as_float(__builtin_amdgcn_update_dpp(0, __float_as_int(mx), 0x128, 0xf, 0xf, false)));
    { auto q = __builtin_amdgcn_permlane16_swap(__float_as_uint(mx), __float_as_uint(mx), false, false); mx = fmaxf(__uint_as_float(q[0]), __uint_as_float(q[1])); }
    int ex = (int)((__float_as_uint(mx) >> 23) & 0xff);
    ex = ex < 20 ? 20 : (ex > 230 ? 230 : ex);
    const float sc = __uint_as_float((unsigned)(127 + 12 + 127 - ex) << 23);
    cb.re *= sc; cb.im *= sc;
    if (wv < 2 && (lane & 31) == 0) scl[mode >> 5] = __uint_as_float((unsigned)(ex - 12) << 23);
    auto cexp = [&](float l) {
      const float e = __builtin_amdgcn_exp2f(a2 * l), ph = __builtin_amdgcn_fractf(br * l);
      return cf{e * __builtin_amdgcn_cosf(ph), e * __builtin_amdgcn_sinf(ph)};
    };
    char* trow = smem + K1_TAB + mode * 4;
    auto put = [&](int row, float re, float im) {
      typedef __attribute__((ext_vector_type(2))) _Float16 f16x2;
      *(f16x2*)(trow + row * KG_RS) = __builtin_convertvector(f32x2{re, im}, f16x2);
    };
    const cf w128 = cexp(128.f);
    if (wv < 2) {
      const cf w1_ = cexp(1.f), w8 = cexp(8.f), w64 = cexp(64.f);
      cf e0 = {1.f, 0.f}, e1 = w64, x = cb;
#pragma unroll
      for (int j = 0; j < 8; ++j) {
        put(j, e0.re, -e0.im); put(8 + j, e1.re, -e1.im);
        const cf y = cmul(x, w128);
        put(16 + j, x.re, x.im); put(24 + j, y.re, y.im);
        if (j < 7) { e0 = cmul(e0, w8); e1 = cmul(e1, w8); x = cmul(x, w1_); }
      }
    } else {
      const cf w2_ = cexp(2.f), w4 = cmul(w2_, w2_);
      cf x = cmul(cb, cexp(256.f));
#pragma unroll
      for (int j = 0; j < 4; ++j) {
        const cf y = cmul(x, w128);
        put(32 + j, x.re, x.im); put(36 + j, y.re, y.im);
        if (j < 3) x = cmul(x, w2_);
      }
      x = cmul(cb, cexp(512.f));
#pragma unroll
      for (int j = 0; j < 4; ++j) {
        const cf y = cmul(x, w4);
        put(40 + 2 * j, x.re, x.im); put(41 + 2 * j, y.re, y.im);
        if (j < 3) x = cmul(x, w128);
      }
      x = cmul(cb, cexp(1024.f));
      cf z = cmul(cb, cexp(1536.f));
#pragma unroll
      for (int j = 0; j < 5; ++j) {
        if (j < 4) put(48 + j, x.re, x.im);
        put(52 + j, z.re, z.im);
        if (j < 4) { x = cmul(x, w128); z = cmul(z, w128); }
      }
    }
  }
  STAMP(2);
  lds_barrier();
  u32x4 rt[3];
  if (d < 576 - RT0) retile2_load(RT0 + d, w2, rt);
  unsigned pf = 0;
  if (d >= 384 && wv < 2) pf = *(const unsigned*)((wv ? w3 : w1) + (size_t)(d - 384) * F + 576 * (lane >> 3) + 64 * (lane & 7));
  __builtin_amdgcn_sched_barrier(0);
  if (wv < 3) {
    const int arow = 16 + 16 * wv + (wv == 2 && p > 8 ? 8 : p);
    f32x4 sum = {0.f, 0.f, 0.f, 0.f};
#pragma unroll
    for (int gq = 0; gq < 4; ++gq) {
      f32x4 acc = {0.f, 0.f, 0.f, 0.f};
#pragma unroll
      for (int kk = 0; kk < 2; ++kk) {
        const char* q = smem + K1_TAB + (32 * gq + 16 * kk + 4 * g) * 4;
        acc = __builtin_amdgcn_mfma_f32_16x16x32_f16(*(const f16x8*)(q + arow * KG_RS), *(const f16x8*)(q + p * KG_RS), acc, 0, 0, 0);
      }
      sum += acc * scl[gq];
    }
    const int b0 = wv == 0 ? 4 * (g & 1) + 128 * (g >> 1) + 8 * p : (wv == 2 ? 512 + 64 * g + p : (g < 2 ? 256 + 64 * g + 4 * p : 256 + 64 * g + 2 * p));
    const int s1 = wv == 2 ? 16 : 1, s2 = (wv == 2 || (wv == 1 && g >= 2)) ? 32 : 2;
#pragma unroll
    for (int r = 0; r < 4; ++r) tab[min(b0 + (r & 1) * s1 + (r >> 1) * s2, 656)] = sum[r];
  }
  const float sa = mv[0], ca1 = rb(1.f + mv[1]), ga = mv[2];
  float h[8];
#pragma unroll
  for (int e = 0; e < 8; ++e) h[e] = rb(rb(rb(rb(elem(xv, e) - (e < 4 ? m0[e & 3] : m1[e & 3])) * (e < 4 ? r0[e & 3] : r1[e & 3])) * ca1) + sa);
  *(u32x4*)(smem + K1_U + 480 + 16 * tid) = u32x4{pk2(h[0], h[1]), pk2(h[2], h[3]), pk2(h[4], h[5]), pk2(h[6], h[7])};
  if (tid < 30) *(u32x4*)(smem + K1_U + 16 * tid) = u32x4{0, 0, 0, 0};
  STAMP(3);
  lds_barrier();
  {
    float kv[12];
#pragma unroll
    for (int i = 0; i < 12; i += 2) {
      const int l = 2047 - (8 * tid + i);
      const unsigned bits = pk2((float)l, (float)(l - 1));
      const int i0 = l < 128 ? (l < 0 ? 0 : l) : (int)(bits & 0xffff) - 17024, i1 = l - 1 < 128 ? (l < 1 ? 0 : l - 1) : (int)(bits >> 16) - 17024;
      kv[i] = tab[i0]; kv[i + 1] = tab[i1];
      if (i >= 8 && tid == 255) kv[i] = kv[i + 1] = 0.f;
    }
    unsigned w[6];
#pragma unroll
    for (int i = 0; i < 6; ++i) w[i] = pk2(kv[2 * i], kv[2 * i + 1]);
    auto al = [](unsigned hi, unsigned lo) { return __builtin_amdgcn_alignbit(hi, lo, 16); };
    char* cp = smem + K1_TAB + 16 * tid;
    *(u32x4*)(cp + 0 * CP_STRIDE) = u32x4{w[0], w[1], w[2], w[3]};
    *(u32x4*)(cp + 1 * CP_STRIDE) = u32x4{al(w[1], w[0]), al(w[2], w[1]), al(w[3], w[2]), al(w[4], w[3])};
    *(u32x4*)(cp + 2 * CP_STRIDE) = u32x4{w[1], w[2], w[3], w[4]};
    *(u32x4*)(cp + 3 * CP_STRIDE) = u32x4{al(w[2], w[1]), al(w[3], w[2]), al(w[4], w[3]), al(w[5], w[4])};
    if (tid < 8) *(u32x4*)(smem + K1_TAB + (tid >> 1) * CP_STRIDE + 4096 + 16 * (tid & 1)) = u32x4{0, 0, 0, 0};
  }
  STAMP(4);
  lds_barrier();
  const int sg = (3 - p) & 3;
  const unsigned abase = K1_TAB + sg * CP_STRIDE + 2 * (2031 + 8 * g - p - sg);
  const unsigned bbase = K1_U + 2 * (240 + 16 * p - 16 + 8 * g);
  f32x4 acc[8] = {};
#pragma unroll
  for (int cc = 0; cc < 2; ++cc) {
    const int c0 = 2 * wv + cc;
    bf16x8 A[8], B[8];
#pragma unroll
    for (int i = 0; i < 8; ++i) {
      const char* pa = smem + abase - 64 * c0 - 512 * i;
      u32x2 lo = *(const u32x2*)pa, hi = *(const u32x2*)(pa + 8);
      A[i] = __builtin_bit_cast(bf16x8, u32x4{lo.x, lo.y, hi.x, hi.y});
      B[i] = *(const bf16x8*)(smem + bbase - 64 * c0 + 512 * i);
    }
#pragma unroll
    for (int c1 = 0; c1 < 8; ++c1)
#pragma unroll
      for (int i = c1; i < 8; ++i) acc[i] = __builtin_amdgcn_mfma_f32_16x16x32_bf16(A[c1], B[i - c1], acc[i], 0, 0, 0);
  }
  float* red = (float*)smem;
  if (d < 576 - RT0) retile_stage<3>(rt, smem + K1_ST);
  STAMP(5);
  lds_barrier();
#pragma unroll
  for (int i = 0; i < 8; ++i) *(f32x4*)(red + wv * 2560 + (16 * i + p) * 20 + 4 * g) = acc[i];
  lds_barrier();
  STAMP(6);
  float y[8];
  {
    const int o = (tid >> 1) * 20 + (tid & 1) * 8;
#pragma unroll
    for (int hh = 0; hh < 2; ++hh) {
      f32x4 a = *(const f32x4*)(red + o + 4 * hh), b = *(const f32x4*)(red + 2560 + o + 4 * hh);
      f32x4 c = *(const f32x4*)(red + 5120 + o + 4 * hh), e = *(const f32x4*)(red + 7680 + o + 4 * hh);
      f32x4 s = (a + b) + (c + e);
#pragma unroll
      for (int k = 0; k < 4; ++k) y[4 * hh + k] = s[k];
    }
  }
  float o[8];
#pragma unroll
  for (int e = 0; e < 8; ++e) { float y2 = rb(rb(y[e]) + rb(h[e] * dsk)); o[e] = elem(xv, e) + rb(ga * y2); }
  *(u32x4*)((u16*)(ws + WS_X1T) + (size_t)d * L + 8 * tid) = u32x4{pk2(o[0], o[1]), pk2(o[2], o[3]), pk2(o[4], o[5]), pk2(o[6], o[7])};
  if (d < 576 - RT0) retile2_emit(RT0 + d, smem + K1_ST, ws + WS_WT);
  if (tid == 0) {
    u16* mf = (u16*)(ws + WS_MODF);
    mf[d] = (u16)pk2(mv[3], 0.f);
    mf[D + d] = (u16)pk2(1.f + mv[4], 0.f);
    mf[2 * D + d] = (u16)pk2(mv[5], 0.f);
  }
  asm volatile("" :: "v"(pf));
  STAMP(7);
}

constexpr int K3_H = 73728, K3_ST = 2 * 73728, K3_LDS = 2 * 73728 + 8192;
#define LDIDX(i) (i)
constexpr int RING = 16;
DEV unsigned swiglu_tile(const f32x4& c) {
  auto s0 = __builtin_amdgcn_permlane32_swap(__float_as_uint(c[0]), __float_as_uint(c[2]), false, false);
  auto s1 = __builtin_amdgcn_permlane32_swap(__float_as_uint(c[1]), __float_as_uint(c[3]), false, false);
  const f32x2 gt = {__uint_as_float(s0[0]), __uint_as_float(s1[0])}, up = {__uint_as_float(s0[1]), __uint_as_float(s1[1])};
  const f32x2 ar = gt * -1.44269504088896341f;
  const f32x2 dn = f32x2{__builtin_amdgcn_exp2f(ar.x), __builtin_amdgcn_exp2f(ar.y)} + 1.f;
  const f32x2 hv = gt * f32x2{__builtin_amdgcn_rcpf(dn.x), __builtin_amdgcn_rcpf(dn.y)} * up;
  return pk2(hv.x, hv.y);
}
DEV bf16x8 tfrag(const u32x4 (&raw)[8], int i) {
  const unsigned sel = (i & 1) ? 0x07060302u : 0x05040100u;
  u32x4 o;
#pragma unroll
  for (int q = 0; q < 4; ++q) o[q] = __builtin_amdgcn_perm(raw[2 * q + 1][i >> 1], raw[2 * q][i >> 1], sel);
  return __builtin_bit_cast(bf16x8, o);
}
template <int NT>
DEV void ffn_phase2(__amdgpu_buffer_rsrc_t rs, int wv, int lane, char* smem, int s, int mt, u16* out, bf16x8 (&ring)[RING], char* ws STAMP_ARG) {
  const unsigned vo = lane * 16;
  constexpr int F0 = NT == 5 ? 36 : 0, NFR = F0 + 18 * NT;
  auto ldw = [&](int idx) { return __builtin_bit_cast(bf16x8, __builtin_amdgcn_raw_buffer_load_b128(rs, vo, idx * 1024, 0)); };
  bf16x8 b[2][4];
  f32x4 oc[NT][4];
#pragma unroll
  for (int tt = 0; tt < 4; ++tt) b[0][tt] = *(const bf16x8*)(smem + K3_H + tt * 1024 + lane * 16);
#pragma unroll
  for (int ks = 0; ks < 18; ++ks) {
    if (ks < 17) {
#pragma unroll
      for (int tt = 0; tt < 4; ++tt) b[(ks + 1) & 1][tt] = *(const bf16x8*)(smem + K3_H + (ks + 1) * 4096 + tt * 1024 + lane * 16);
    }
#pragma unroll
    for (int ti = 0; ti < NT; ++ti) {
      const int i = F0 + ks * NT + ti;
#pragma unroll
      for (int tt = 0; tt < 4; ++tt)
        oc[ti][tt] = __builtin_amdgcn_mfma_f32_16x16x32_bf16(ring[i % RING], b[ks & 1][tt], ks == 0 ? f32x4{0.f, 0.f, 0.f, 0.f} : oc[ti][tt], 0, 0, 0);
      if (i + RING < NFR) ring[i % RING] = ldw(i + RING);
      __builtin_amdgcn_sched_barrier(0);
    }
  }
  STAMP(8);
  const int dt0 = NT == 5 ? 5 * wv : 20 + 4 * (wv - 4);
  char* pbase = ws + WS_P + (size_t)mt * 8 * 73728;
  __amdgpu_buffer_rsrc_t rp = __builtin_amdgcn_make_buffer_rsrc(pbase, 0, 8 * 73728, 0x00020000);
#pragma unroll
  for (int ti = 0; ti < NT; ++ti)
#pragma unroll
    for (int tp = 0; tp < 2; ++tp) {
      const f32x4 u = oc[ti][2 * tp], v = oc[ti][2 * tp + 1];
      __builtin_amdgcn_raw_buffer_store_b128(u32x4{pk2(u[0], u[1]), pk2(u[2], u[3]), pk2(v[0], v[1]), pk2(v[2], v[3])}, rp, vo,
                                             s * 73728 + ((dt0 + ti) * 2 + tp) * 1024, 16  );
    }
  const u16* x1T = (const u16*)(ws + WS_X1T);
  const u16* mf = (const u16*)(ws + WS_MODF);
  constexpr int NU = NT == 5 ? 1 : 2;
  const int nu = wv == 7 ? 2 : 1;
  unsigned xr[NU][8], gb[NU][2];
#pragma unroll
  for (int k = 0; k < NU; ++k)
    if (k < nu) {
      const int un = 9 * s + wv + k, d0 = 16 * (un >> 1) + 4 * (lane >> 4), t0 = 64 * mt + 32 * (un & 1) + (lane & 15);
#pragma unroll
      for (int e = 0; e < 4; ++e) { xr[k][e] = x1T[(size_t)(d0 + e) * L + t0]; xr[k][4 + e] = x1T[(size_t)(d0 + e) * L + t0 + 16]; }
      const u32x2 gg = *(const u32x2*)(mf + 2 * D + d0);
      gb[k][0] = gg.x; gb[k][1] = gg.y;
    }
  asm volatile("s_waitcnt vmcnt(0)" ::: "memory");
  STAMP(9);
  __builtin_amdgcn_s_barrier();
  {
    const char* cnt = ws + WS_CNT + mt * 128;
    unsigned zero = 0, one = 1, tmp, sv, left;
    unsigned long long ex;
    asm volatile(
        "s_cmp_lg_u32 %[wv], 0\n\t"
        "s_cbranch_scc1 2f\n\t"
        "s_mov_b64 %[ex], exec\n\t"
        "s_mov_b64 exec, 1\n\t"
        "s_nop 1\n\t"
        "global_atomic_add %[zero], %[one], %[cnt]\n\t"
        "s_mov_b32 %[left], 0x100000\n\t"
        "1:\n\t"
        "global_load_dword %[tmp], %[zero], %[cnt] sc1\n\t"
        "s_waitcnt vmcnt(0)\n\t"
        "v_readfirstlane_b32 %[sv], %[tmp]\n\t"
        "s_cmp_ge_u32 %[sv], 8\n\t"
        "s_cbranch_scc1 3f\n\t"
        "s_sleep 2\n\t"
        "s_sub_u32 %[left], %[left], 1\n\t"
        "s_cmp_lg_u32 %[left], 0\n\t"
        "s_cbranch_scc1 1b\n\t"
        "3:\n\t"
        "s_mov_b64 exec, %[ex]\n\t"
        "2:\n\t"
        : [tmp] "=&v"(tmp), [sv] "=&s"(sv), [left] "=&s"(left), [ex] "=&s"(ex)
        : [wv] "s"(wv), [zero] "v"(zero), [one] "v"(one), [cnt] "s"(cnt)
        : "memory", "scc");
  }
  asm volatile("" ::: "memory");
  __builtin_amdgcn_s_barrier();
  asm volatile("" ::: "memory");
  STAMP(10);
  u32x4 pv[NU][8];
#pragma unroll
  for (int k = 0; k < NU; ++k)
    if (k < nu)
#pragma unroll
      for (int q = 0; q < 8; ++q) pv[k][q] = __builtin_amdgcn_raw_buffer_load_b128(rp, vo, q * 73728 + (9 * s + wv + k) * 1024, 16  );
#pragma unroll
  for (int k = 0; k < NU; ++k)
    if (k < nu) {
      const int un = 9 * s + wv + k, d0 = 16 * (un >> 1) + 4 * (lane >> 4), t0 = 64 * mt + 32 * (un & 1) + (lane & 15);
      float o[8];
#pragma unroll
      for (int e = 0; e < 8; ++e) {
        const float a = ((elem(pv[k][0], e) + elem(pv[k][1], e)) + (elem(pv[k][2], e) + elem(pv[k][3], e))) +
                        ((elem(pv[k][4], e) + elem(pv[k][5], e)) + (elem(pv[k][6], e) + elem(pv[k][7], e)));
        const unsigned gw = gb[k][(e & 3) >> 1];
        o[e] = bf2f(xr[k][e]) + rb(((e & 1) ? bfhi(gw) : bflo(gw)) * rb(a));
      }
      *(u32x2*)(out + (size_t)t0 * D + d0) = u32x2{pk2(o[0], o[1]), pk2(o[2], o[3])};
      *(u32x2*)(out + (size_t)(t0 + 16) * D + d0) = u32x2{pk2(o[4], o[5]), pk2(o[6], o[7])};
    }
  STAMP(11);
  STAMP_OUT(ws, 12);
}

DEV void ffn_phase1(const char* wp, const char* whead, const char* pfp, __amdgpu_buffer_rsrc_t rs, int wv, int lane, char* smem, u32x4 (&raw)[2][8], int s, int mt, u16* out, char* ws STAMP_ARG) {
  bf16x8 ring[RING];
  const unsigned vo = lane * 16;
  auto ldw = [&](int idx) { return __builtin_bit_cast(bf16x8, __builtin_amdgcn_raw_buffer_load_b128(rs, vo, idx * 1024, 0)); };
  unsigned boff[4];
#pragma unroll
  for (int tt = 0; tt < 4; ++tt) boff[tt] = tt * 1024 + ((lane ^ ((2 * tt + ((lane >> 3) & 1)) & 7)) * 16);
  f32x4 acc[8][4] = {};
  bf16x8 b[4];
#pragma unroll
  for (int tt = 0; tt < 4; ++tt) b[tt] = *(const bf16x8*)(smem + boff[tt]);
  bf16x8 a = tfrag(raw[0], 0);
  auto ldrow = [&](int j) {
    const char* p;
    asm("v_lshl_add_u64 %0, %1, 0, %2" : "=v"(p) : "v"(wp), "s"((unsigned long long)j * (F * 2)));
    return *(const __attribute__((address_space(1))) u32x4*)p;
  };
#define SGB(m, n) __builtin_amdgcn_sched_group_barrier(m, n, 0)
  auto step = [&](int par, int ksn, unsigned inc) {
    bf16x8 hi[4];
#pragma unroll
    for (int i = 0; i < 4; ++i) {
      bf16x8 an;
      if (i < 3) an = tfrag(raw[par], i + 1);
      hi[i] = tfrag(raw[par], 4 + i);
#pragma unroll
      for (int tt = 0; tt < 4; ++tt) acc[i][tt] = __builtin_amdgcn_mfma_f32_16x16x32_bf16(a, b[tt], acc[i][tt], 0, 0, 0);
      if (i < 3) a = an;
      SGB(0x8, 1); SGB(0x2, 2); SGB(0x8, 1); SGB(0x2, 2); SGB(0x8, 1); SGB(0x2, 2); SGB(0x8, 1); SGB(0x2, 2);
      __builtin_amdgcn_sched_barrier(0);
    }
#pragma unroll
    for (int i = 4; i < 6; ++i) {
#pragma unroll
      for (int j = 0; j < 3; ++j) raw[par][3 * (i - 4) + j] = ldrow(3 * (i - 4) + j);
#pragma unroll
      for (int tt = 0; tt < 4; ++tt) acc[i][tt] = __builtin_amdgcn_mfma_f32_16x16x32_bf16(hi[i - 4], b[tt], acc[i][tt], 0, 0, 0);
      SGB(0x8, 1); SGB(0x2, 1); SGB(0x20, 1); SGB(0x8, 1); SGB(0x2, 1); SGB(0x20, 1); SGB(0x8, 1); SGB(0x2, 1); SGB(0x20, 1); SGB(0x8, 1);
      __builtin_amdgcn_sched_barrier(0);
    }
    const char* bn = smem + ksn * 4096;
#pragma unroll
    for (int tt = 0; tt < 4; ++tt) {
      if (tt < 2) raw[par][6 + tt] = ldrow(6 + tt);
      if (tt == 2) { a = tfrag(raw[par ^ 1], 0); wp += inc; }
      acc[6][tt] = __builtin_amdgcn_mfma_f32_16x16x32_bf16(hi[2], b[tt], acc[6][tt], 0, 0, 0);
      acc[7][tt] = __builtin_amdgcn_mfma_f32_16x16x32_bf16(hi[3], b[tt], acc[7][tt], 0, 0, 0);
      b[tt] = *(const bf16x8*)(bn + boff[tt]);
      SGB(0x8, 1); SGB(0x2, 3); SGB(0x20, 1); SGB(0x8, 1); SGB(0x2, 3); SGB(0x100, 1);
      __builtin_amdgcn_sched_barrier(0);
    }
  };
  unsigned pf = 0;
#pragma unroll 1
  for (int it = 0; it < 9; ++it) {
    asm volatile("" :: "v"(pf));
    if (it < 6 && wv == it) pf = *(const unsigned*)(pfp + (size_t)it * 64 * (F * 2));
    const bool last = it == 8;
    if (last) wp = whead;
    const unsigned inc = (last ? 8 : 32) * (F * 2);
    step(0, 2 * it + 1, inc);
    step(1, last ? 0 : 2 * it + 2, inc);
  }
#pragma unroll
  for (int i = 0; i < 16; ++i) ring[i] = __builtin_bit_cast(bf16x8, raw[i >> 3][i & 7]);
  STAMP(5);
  char* hw0 = smem + K3_H + (2 * wv) * 4096 + lane * 16;
  auto put4 = [&](int h, int tt) {
    *(u32x4*)(hw0 + h * 4096 + tt * 1024) = u32x4{swiglu_tile(acc[4 * h][tt]), swiglu_tile(acc[4 * h + 1][tt]), swiglu_tile(acc[4 * h + 2][tt]), swiglu_tile(acc[4 * h + 3][tt])};
  };
  if (wv < 4) {
    put4(0, 0); put4(0, 1);
    __builtin_amdgcn_sched_barrier(0);
    f32x4 am[2][4] = {};
#pragma unroll
    for (int ks = 0; ks < 18; ++ks) {
#pragma unroll
      for (int tt = 0; tt < 4; ++tt) {
        am[0][tt] = __builtin_amdgcn_mfma_f32_16x16x32_bf16(ring[(2 * ks) % RING], b[tt], am[0][tt], 0, 0, 0);
        am[1][tt] = __builtin_amdgcn_mfma_f32_16x16x32_bf16(ring[(2 * ks + 1) % RING], b[tt], am[1][tt], 0, 0, 0);
        if (ks < 17) b[tt] = *(const bf16x8*)(smem + (ks + 1) * 4096 + boff[tt]);
      }
      ring[(2 * ks) % RING] = ldw(2 * ks + RING);
      ring[(2 * ks + 1) % RING] = ldw(2 * ks + 1 + RING);
      if (ks < 18 && ks % 3 == 0) put4((ks / 3 + 2) >> 2, (ks / 3 + 2) & 3);
      __builtin_amdgcn_sched_barrier(0);
    }
    char* hw8 = smem + K3_H + (16 + (wv >> 1)) * 4096 + lane * 16 + (wv & 1) * 8;
#pragma unroll
    for (int tt = 0; tt < 4; ++tt) *(u32x2*)(hw8 + tt * 1024) = u32x2{swiglu_tile(am[0][tt]), swiglu_tile(am[1][tt])};
    STAMP(6);
    __syncthreads();
    STAMP(7);
    ffn_phase2<5>(rs, wv, lane, smem, s, mt, out, ring, ws STAMP_PASS);
    __builtin_amdgcn_endpgm();
  }
#pragma unroll
  for (int q = 0; q < 8; ++q) put4(q >> 2, q & 3);
  STAMP(6);
  __syncthreads();
  STAMP(7);
  ffn_phase2<4>(rs, wv, lane, smem, s, mt, out, ring, ws STAMP_PASS);
  __builtin_amdgcn_endpgm();
}
__global__ __launch_bounds__(512, 2) void k3_ffn(const u16* w1, const u16* w3, char* ws, u16* out) {
  extern __shared__ __attribute__((aligned(16))) char smem[];
  const int tid = threadIdx.x, lane = tid & 63;
  const int wv = __builtin_amdgcn_readfirstlane(tid >> 6);
  STAMP_DECL
  STAMP(0);
  const int xc = blockIdx.x & 7, bi = blockIdx.x >> 3;
  const int s = (xc % (8 / SPX)) * SPX + bi % SPX, mt = (xc / (8 / SPX)) * (32 / SPX) + bi / SPX;
  const u16* x1T = (const u16*)(ws + WS_X1T) + 64 * mt;
  const u16* mf = (const u16*)(ws + WS_MODF);
  const int tc = tid & 7, cg = tid >> 3;
  u32x4 r[9];
#pragma unroll
  for (int e = 0; e < 8; ++e) r[e] = *(const u32x4*)(x1T + (size_t)(8 * cg + e) * L + 8 * tc);
  r[8] = *(const u32x4*)(x1T + (size_t)(512 + cg) * L + 8 * tc);
  const u32x4 sbv = *(const u32x4*)(mf + 8 * cg), cbv = *(const u32x4*)(mf + D + 8 * cg);
  const float sb8 = bf2f(mf[512 + cg]), cb8 = bf2f(mf[D + 512 + cg]);
  __builtin_amdgcn_sched_barrier(0);
  asm volatile("s_waitcnt vmcnt(0)" ::: "memory");
  STAMP(1);
  __builtin_amdgcn_sched_barrier(0);
  f32x2 xv[9][4];
#pragma unroll
  for (int e = 0; e < 9; ++e)
#pragma unroll
    for (int t = 0; t < 4; ++t) xv[e][t] = f32x2{bflo(r[e][t]), bfhi(r[e][t])};
  float s1, s2;
  {
    f32x2 p1[4], p2[4];
#pragma unroll
    for (int t = 0; t < 4; ++t) {
      p1[t] = xv[0][t]; p2[t] = xv[0][t] * xv[0][t];
#pragma unroll
      for (int e = 1; e < 9; ++e) { p1[t] += xv[e][t]; p2[t] += xv[e][t] * xv[e][t]; }
    }
    auto sw32 = [](float a, float b) { auto q = __builtin_amdgcn_permlane32_swap(__float_as_uint(a), __float_as_uint(b), false, false); return __uint_as_float(q[0]) + __uint_as_float(q[1]); };
    auto sw16 = [](float a, float b) { auto q = __builtin_amdgcn_permlane16_swap(__float_as_uint(a), __float_as_uint(b), false, false); return __uint_as_float(q[0]) + __uint_as_float(q[1]); };
    float u1[4], u2[4];
#pragma unroll
    for (int t = 0; t < 4; ++t) { u1[t] = sw32(p1[t >> 1][t & 1], p1[2 + (t >> 1)][t & 1]); u2[t] = sw32(p2[t >> 1][t & 1], p2[2 + (t >> 1)][t & 1]); }
    const float v10 = sw16(u1[0], u1[2]), v11 = sw16(u1[1], u1[3]), v20 = sw16(u2[0], u2[2]), v21 = sw16(u2[1], u2[3]);
    const bool b3 = lane & 8;
    auto ror8 = [](float v) { return __int_as_float(__builtin_amdgcn_update_dpp(0, __float_as_int(v), 0x128, 0xf, 0xf, false)); };
    s1 = (b3 ? v11 : v10) + ror8(b3 ? v10 : v11);
    s2 = (b3 ? v21 : v20) + ror8(b3 ? v20 : v21);
  }
  float* st = (float*)(smem + K3_ST);
  *(f32x2*)(st + (wv * 64 + 8 * tc + (lane >> 3)) * 2) = f32x2{s1, s2};
  __builtin_amdgcn_sched_barrier(0);
  char* wbase = ws + WS_WT + ((size_t)s * FR_S + wave_frag_off(wv)) * 1024;
  __amdgpu_buffer_rsrc_t rs = __builtin_amdgcn_make_buffer_rsrc(wbase, 0, 126 * 1024, 0x00020000);
  const char* wp = (const char*)((lane & 8) ? w3 : w1) + ((size_t)(8 * (lane >> 4)) * F + 576 * s + 64 * wv + 8 * (lane & 7)) * 2;
  u32x4 raw[2][8];
#pragma unroll
  for (int i = 0; i < 2; ++i) {
#pragma unroll
    for (int j = 0; j < 8; ++j) raw[i][j] = *(const u32x4*)(wp + (size_t)j * (F * 2));
    wp += (size_t)32 * (F * 2);
  }
  __builtin_amdgcn_sched_barrier(0);
  STAMP(2);
  asm volatile("s_waitcnt lgkmcnt(0)" ::: "memory");
  __builtin_amdgcn_s_barrier();
  STAMP(3);
  float* sw = (float*)(smem + K3_ST + 4096) + wv * 128;
  {
    float a = 0.f, b = 0.f;
#pragma unroll
    for (int w = 0; w < 8; ++w) { f32x2 v = *(const f32x2*)(st + (w * 64 + lane) * 2); a += v.x; b += v.y; }
    const float mean = a * (1.f / D);
    const float var = fmaxf(b * (1.f / D) - mean * mean, 0.f);
    const float rd = 1.f / rb(sqrtf(rb(rb(var) + 1e-5f)));
    sw[lane] = rd; sw[64 + lane] = -rb(mean) * rd;
  }
  const f32x4 rd0 = *(const f32x4*)(sw + 8 * tc), rd1 = *(const f32x4*)(sw + 8 * tc + 4);
  const f32x4 nm0 = *(const f32x4*)(sw + 64 + 8 * tc), nm1 = *(const f32x4*)(sw + 64 + 8 * tc + 4);
  const f32x2 rdp[4] = {{rd0[0], rd0[1]}, {rd0[2], rd0[3]}, {rd1[0], rd1[1]}, {rd1[2], rd1[3]}};
  const f32x2 nmp[4] = {{nm0[0], nm0[1]}, {nm0[2], nm0[3]}, {nm1[0], nm1[1]}, {nm1[2], nm1[3]}};
  char* base = smem + (cg >> 2) * 4096 + (tc >> 1) * 1024 + (cg & 3) * 256 + (tc & 1) * 128;
  char* base8 = smem + (16 + (wv >> 2)) * 4096 + (tc >> 1) * 1024 + (wv & 3) * 256 + (tc & 1) * 128 + (cg & 7) * 2;
#pragma unroll
  for (int t = 0; t < 4; ++t) {
    f32x2 hv[9];
#pragma unroll
    for (int e = 0; e < 9; ++e) {
      const float cb = e < 8 ? elem(cbv, e) : cb8, sb = e < 8 ? elem(sbv, e) : sb8;
      hv[e] = __builtin_elementwise_fma(__builtin_elementwise_fma(xv[e][t], rdp[t], nmp[t]), f32x2{cb, cb}, f32x2{sb, sb});
    }
#pragma unroll
    for (int p = 0; p < 2; ++p) {
      *(u32x4*)(base + (((2 * t + p) ^ tc) * 16)) = u32x4{pk2(hv[0][p], hv[1][p]), pk2(hv[2][p], hv[3][p]), pk2(hv[4][p], hv[5][p]), pk2(hv[6][p], hv[7][p])};
      *(u16*)(base8 + (((2 * t + p) ^ tc) * 16)) = (u16)pk2(hv[8][p], 0.f);
    }
  }
  asm volatile("s_waitcnt lgkmcnt(0)" ::: "memory");
  __builtin_amdgcn_s_barrier();
  STAMP(4);
  const char* pfp = (const char*)((lane & 8) ? w3 : w1) + ((size_t)(192 + 4 * (bi / SPX) + (lane >> 4)) * F + 576 * s + 64 * (lane & 7)) * 2;
  ffn_phase1(wp, ws + WS_WT + ((size_t)s * FR_S + wv) * 1024 + lane * 16, pfp, rs, wv, lane, smem, raw, s, mt, out, ws STAMP_PASS);
}

extern "C" void kernel_launch(void* const* d_in, const int* in_sizes, int n_in, void* d_out, int out_size, void* d_ws,
                              size_t ws_size, hipStream_t stream) {
  const u16* x = (const u16*)d_in[0];
  const u16 *Are = (const u16*)d_in[1], *Aim = (const u16*)d_in[2], *Bre = (const u16*)d_in[3], *Bim = (const u16*)d_in[4];
  const u16 *Cre = (const u16*)d_in[5], *Cim = (const u16*)d_in[6], *logdt = (const u16*)d_in[7], *Dskip = (const u16*)d_in[8];
  const u16 *w1 = (const u16*)d_in[10], *w3 = (const u16*)d_in[11], *w2 = (const u16*)d_in[12];
  const u16 *c = (const u16*)d_in[13], *wmod = (const u16*)d_in[14];
  char* ws = (char*)d_ws;
  (void)hipFuncSetAttribute((const void*)k3_ffn, hipFuncAttributeMaxDynamicSharedMemorySize, K3_LDS);
  k0_prep<<<240 + RT0, 256, K0_LDS, stream>>>(x, w1, w3, w2, c, wmod, ws);
  k1_conv<<<D, 256, K1_LDS, stream>>>(Are, Aim, Bre, Bim, Cre, Cim, logdt, Dskip, w1, w3, w2, ws);
  k3_ffn<<<256, 512, K3_LDS, stream>>>(w1, w3, ws, (u16*)d_out);
}
```

```cpp
#include <hip/hip_runtime.h>
#include <hip/hip_bf16.h>
#include <stdint.h>

typedef __attribute__((ext_vector_type(8))) __bf16 bf16x8;
typedef __attribute__((ext_vector_type(2))) __bf16 bf16x2;
typedef __attribute__((ext_vector_type(8))) _Float16 f16x8;
typedef __attribute__((ext_vector_type(4))) float f32x4;
typedef __attribute__((ext_vector_type(2))) float f32x2;
typedef __attribute__((ext_vector_type(4))) unsigned u32x4;
typedef __attribute__((ext_vector_type(2))) unsigned u32x2;
typedef __attribute__((ext_vector_type(4))) short s16x4;
typedef unsigned short u16;
#define LDSP __attribute__((address_space(3)))
#define DEV __device__ __forceinline__

#define STAMP(i)
#define STAMP_DECL
#define STAMP_ARG
#define STAMP_PASS
#define STAMP_OUT(ws, n)
#define KT_BEGIN(k)
#define KT_END(k, ws)
#define K2PRIO 1
#define PREF1 0
#define LBAR 0
#define PREF 1
#define PREF2 0
#define SPX 2
constexpr int L = 2048, D = 576, NM = 128, F = 4608;
constexpr int KR_STRIDE = 2064;
constexpr int MODK = 8;
constexpr int FR_S = 144 + 664;

constexpr size_t WS_MODP = 0;
constexpr size_t WS_MODF = 114688;
constexpr size_t WS_CNT  = 118784;
constexpr size_t WS_XT   = 131072;
constexpr size_t WS_ZT   = WS_XT + (size_t)D * L * 2;
constexpr size_t WS_X1T  = WS_ZT + (size_t)D * L * 2;
constexpr size_t WS_KR   = WS_X1T + (size_t)D * L * 2;
constexpr size_t WS_WT   = (WS_KR + (size_t)D * KR_STRIDE * 2 + 4095) / 4096 * 4096;
constexpr size_t WS_P    = WS_WT + (size_t)8 * FR_S * 1024;

DEV float bf2f(unsigned v) { return __uint_as_float(v << 16); }
DEV float bflo(unsigned v) { return __uint_as_float(v << 16); }
DEV float bfhi(unsigned v) { return __uint_as_float(v & 0xffff0000u); }
DEV float rb(float x) { return (float)(__bf16)x; }
DEV unsigned pk2(float a, float b) { f32x2 v = {a, b}; bf16x2 r = __builtin_convertvector(v, bf16x2); return __builtin_bit_cast(unsigned, r); }
DEV float elem(const u32x4& v, int i) { unsigned w = v[i >> 1]; return (i & 1) ? bfhi(w) : bflo(w); }
DEV int wave_frag_off(int w) { return 144 - 16 + (w < 4 ? 110 * w : 440 + 56 * (w - 4)); }
DEV int frag_pos(int s, int w, int i) { return s * FR_S + (i < 16 ? i * 9 + w : wave_frag_off(w) + i); }

DEV void lds_barrier() { asm volatile("s_waitcnt lgkmcnt(0)" ::: "memory"); __builtin_amdgcn_s_barrier(); asm volatile("" ::: "memory"); }
DEV void retile1_load(int u, const u16* w1, const u16* w3, u32x4 (&r)[3]) {
  const int tid = threadIdx.x & 255, s = u & 7, ks = u >> 3;
  const size_t o = (size_t)(32 * ks + (tid >> 3)) * F + 576 * s + 512 + (tid & 7) * 8;
  r[0] = *(const u32x4*)(w1 + o);
  r[1] = *(const u32x4*)(w3 + o);
}
DEV void retile2_load(int u, const u16* w2, u32x4 (&r)[3]) {
  const int tid = threadIdx.x & 255, s = u & 7, v = (u >> 3) - 18, ks = (v * 43) >> 7, th = v - 3 * ks;
  const int kk = tid >> 3, gg = kk >> 3, jj = kk & 7, cp = 4 * (gg & 1) + 2 * (gg >> 1) + (jj & 1);
  const bool nrm = ks < 16;
  const int f = (nrm ? 64 * (ks >> 1) + 4 * (ks & 1) : 32 * ks) + (nrm ? 8 * cp + (jj >> 1) : cp + 8 * (jj >> 1));
  const u16* p = w2 + (size_t)(576 * s + f) * D + 192 * th + (tid & 7) * 8;
#pragma unroll
  for (int i = 0; i < 3; ++i) r[i] = *(const u32x4*)(p + 64 * i);
}
template <int N> DEV void retile_stage(const u32x4 (&r)[3], char* st) {
  const int tid = threadIdx.x & 255;
  if (N == 2) { *(u32x4*)(st + tid * 16) = r[0]; *(u32x4*)(st + 4096 + tid * 16) = r[1]; }
  else {
#pragma unroll
    for (int i = 0; i < 3; ++i) *(u32x4*)(st + (tid >> 3) * 384 + (tid & 7) * 16 + 128 * i) = r[i];
  }
}
template <int RS> DEV u32x4 tr_frag(const char* p) {
  s16x4 lo = __builtin_amdgcn_ds_read_tr16_b64_v4i16((LDSP s16x4*)p);
  s16x4 hi = __builtin_amdgcn_ds_read_tr16_b64_v4i16((LDSP s16x4*)(p + 4 * RS));
  u32x2 l2 = __builtin_bit_cast(u32x2, lo), h2 = __builtin_bit_cast(u32x2, hi);
  return u32x4{l2.x, l2.y, h2.x, h2.y};
}
DEV void retile1_emit(int u, const char* st, char* wt) {
  const int lane = threadIdx.x & 63, wv = __builtin_amdgcn_readfirstlane((threadIdx.x >> 6) & 3);
  const int s = u & 7, ks = u >> 3;
  const int g = lane >> 4, qq = (lane & 15) >> 2, pp = lane & 3;
  const char* p = st + (pp >> 1) * 4096 + (8 * g + qq) * 128 + (pp & 1) * 8 + wv * 32;
#pragma unroll
  for (int i = 0; i < 2; ++i)
    *(u32x4*)(wt + (size_t)frag_pos(s, wv, 2 * ks + i) * 1024 + lane * 16) = tr_frag<128>(p + i * 16);
}
DEV void retile2_emit(int u, const char* st, char* wt) {
  const int lane = threadIdx.x & 63, wv = __builtin_amdgcn_readfirstlane((threadIdx.x >> 6) & 3);
  const int s = u & 7, v = (u >> 3) - 18, ks = (v * 43) >> 7, th = v - 3 * ks;
  const int g = lane >> 4, qq = (lane & 15) >> 2, pp = lane & 3;
  const char* p = st + (8 * g + qq) * 384 + pp * 8 + wv * 96;
#pragma unroll
  for (int i = 0; i < 3; ++i) {
    const int dt = 12 * th + wv * 3 + i;
    const int wa = (dt * 13) >> 6, pa = 128 + 110 * wa + 36 + ks * 5 + (dt - 5 * wa);
    const int wb = (dt - 20) >> 2, ib = ks * 4 + ((dt - 20) & 3);
    const int pb = ib < 16 ? ib * 9 + 4 + wb : 128 + 440 + 56 * wb + ib;
    const int pos = __builtin_amdgcn_readfirstlane(s * FR_S + (dt < 20 ? pa : pb));
    *(u32x4*)(wt + (size_t)pos * 1024 + lane * 16) = tr_frag<384>(p + i * 32);
  }
}

#define RT_K1 144
struct cf { float re, im; };
DEV cf cmul(cf a, cf b) {
  cf r;
  asm("v_mul_f32 %0, %2, %4\n\tv_mul_f32 %1, %2, %5\n\tv_fma_f32 %0, -%3, %5, %0\n\tv_fma_f32 %1, %3, %4, %1"
      : "=&v"(r.re), "=&v"(r.im) : "v"(a.re), "v"(a.im), "v"(b.re), "v"(b.im));
  return r;
}
constexpr int LN_RS = 1152 + 16;
DEV void ln1_block(int u, const u16* x, u16* xT, float* stat, char* smem) {
  const int tid = threadIdx.x & 255, tok = tid >> 4, c16 = tid & 15, lane = tid & 63, wv = __builtin_amdgcn_readfirstlane(tid >> 6);
  const int t0 = 16 * ((u & 7) * 16 + (u >> 3));
  const u16* xr = x + (size_t)(t0 + tok) * D + c16 * 8;
  u32x4 r[5];
#pragma unroll
  for (int i = 0; i < 4; ++i) r[i] = *(const u32x4*)(xr + 128 * i);
  r[4] = c16 < 8 ? *(const u32x4*)(xr + 512) : u32x4{0, 0, 0, 0};
#pragma unroll
  for (int i = 0; i < 4; ++i) *(u32x4*)(smem + tok * LN_RS + (c16 + 16 * i) * 16) = r[i];
  if (c16 < 8) *(u32x4*)(smem + tok * LN_RS + (c16 + 64) * 16) = r[4];
  f32x2 sp = {0.f, 0.f};
#pragma unroll
  for (int i = 0; i < 5; ++i)
#pragma unroll
    for (int e = 0; e < 4; ++e) sp += f32x2{bflo(r[i][e]), bfhi(r[i][e])};
  auto sum16 = [](float v) {
    v += __int_as_float(__builtin_amdgcn_update_dpp(0, __float_as_int(v), 0xB1, 0xf, 0xf, false));
    v += __int_as_float(__builtin_amdgcn_update_dpp(0, __float_as_int(v), 0x4E, 0xf, 0xf, false));
    v += __int_as_float(__builtin_amdgcn_update_dpp(0, __float_as_int(v), 0x124, 0xf, 0xf, false));
    v += __int_as_float(__builtin_amdgcn_update_dpp(0, __float_as_int(v), 0x128, 0xf, 0xf, false));
    return v;
  };
  const float mean = sum16(sp.x + sp.y) * (1.f / D);
  f32x2 qp = {0.f, 0.f};
#pragma unroll
  for (int i = 0; i < 5; ++i)
#pragma unroll
    for (int e = 0; e < 4; ++e) {
      f32x2 dl = f32x2{bflo(r[i][e]), bfhi(r[i][e])} - mean;
      if (i == 4 && c16 >= 8) dl = f32x2{0.f, 0.f};
      qp += dl * dl;
    }
  const float q = sum16(qp.x + qp.y);
  if (c16 == 0) { stat[t0 + tok] = rb(mean); stat[L + t0 + tok] = 1.f / rb(sqrtf(rb(rb(q * (1.f / D)) + 1e-5f))); }
  __syncthreads();
  const int g = lane >> 4, qq = (lane & 15) >> 2, pp = lane & 3;
  const char* p = smem + (8 * (g & 1) + qq) * LN_RS + pp * 8 + (g >> 1) * 32 + wv * 64;
  u16* o = xT + (size_t)(32 * wv + 16 * (g >> 1) + (lane & 15)) * L + t0 + 8 * (g & 1);
#pragma unroll
  for (int i = 0; i < 5; ++i)
    if (i < 4 || wv < 2) *(u32x4*)(o + (size_t)i * 128 * L) = tr_frag<LN_RS>(p + i * 256);
}

DEV void mod_block(int b, const u16* c, const u16* wmod, float* modp, char* smem) {
  const int tid = threadIdx.x & 255, c16 = tid & 15, rg = tid >> 4, lane = tid & 63, wv = tid >> 6;
  const int cg = b % 27, kq = b / 27;
  u32x4 r[5]; unsigned cv[5];
#pragma unroll
  for (int i = 0; i < 5; ++i) {
    const int k = 72 * kq + (i < 4 || rg < 8 ? rg + 16 * i : 0);
    r[i] = *(const u32x4*)(wmod + (size_t)k * 3456 + 128 * cg + 8 * c16);
    cv[i] = c[k];
  }
  float acc[8] = {};
#pragma unroll
  for (int i = 0; i < 5; ++i) {
    const float x = bf2f(cv[i]);
    const float sv = i < 4 || rg < 8 ? rb(x / (1.f + __expf(-x))) : 0.f;
#pragma unroll
    for (int e = 0; e < 8; ++e) acc[e] += sv * elem(r[i], e);
  }
#pragma unroll
  for (int e = 0; e < 8; ++e) { acc[e] += __shfl_xor(acc[e], 16); acc[e] += __shfl_xor(acc[e], 32); }
  float* red = (float*)smem;
  if (lane < 16)
#pragma unroll
    for (int e = 0; e < 8; ++e) red[wv * 128 + lane * 8 + e] = acc[e];
  __syncthreads();
  if (tid < 128) modp[kq * 3456 + 128 * cg + tid] = (red[tid] + red[128 + tid]) + (red[256 + tid] + red[384 + tid]);
}

constexpr int KG_RS = 528;
DEV void kgen_block(int d, const u16* Are, const u16* Aim, const u16* Bre, const u16* Bim, const u16* Cre, const u16* Cim,
                    const u16* logdt, float* ktab, char* smem) {
  const int tid = threadIdx.x, lane = tid & 63, wv = __builtin_amdgcn_readfirstlane(tid >> 6), p = lane & 15, g = lane >> 4;
  float* scl = (float*)(smem + 57 * KG_RS);
  const int mode = tid & 127, n = d * NM + mode;
  const unsigned iare = Are[n], iaim = Aim[n], ibre = Bre[n], ibim = Bim[n], icre = Cre[n], icim = Cim[n], ildt = logdt[d];
  {
    const float dt = rb(expf(bf2f(ildt)));
    const float a2 = dt * bf2f(iare) * 1.44269504088896341f, br = dt * bf2f(iaim) * 0.15915494309189535f;
    cf cb = cmul(cf{bf2f(icre), bf2f(icim)}, cf{dt * bf2f(ibre), dt * bf2f(ibim)});
    float mx = fmaxf(fabsf(cb.re), fabsf(cb.im));
    mx = fmaxf(mx, __int_as_float(__builtin_amdgcn_update_dpp(0, __float_as_int(mx), 0xB1, 0xf, 0xf, false)));
    mx = fmaxf(mx, __int_as_float(__builtin_amdgcn_update_dpp(0, __float_as_int(mx), 0x4E, 0xf, 0xf, false)));
    mx = fmaxf(mx, __int_as_float(__builtin_amdgcn_update_dpp(0, __float_as_int(mx), 0x124, 0xf, 0xf, false)));
    mx = fmaxf(mx, __int_as_float(__builtin_amdgcn_update_dpp(0, __float_as_int(mx), 0x128, 0xf, 0xf, false)));
    { auto q = __builtin_amdgcn_permlane16_swap(__float_as_uint(mx), __float_as_uint(mx), false, false); mx = fmaxf(__uint_as_float(q[0]), __uint_as_float(q[1])); }
    int ex = (int)((__float_as_uint(mx) >> 23) & 0xff);
    ex = ex < 20 ? 20 : (ex > 230 ? 230 : ex);
    const float sc = __uint_as_float((unsigned)(127 + 12 + 127 - ex) << 23);
    cb.re *= sc; cb.im *= sc;
    if (wv < 2 && (lane & 31) == 0) scl[mode >> 5] = __uint_as_float((unsigned)(ex - 12) << 23);
    auto cexp = [&](float l) {
      const float e = __builtin_amdgcn_exp2f(a2 * l), ph = __builtin_amdgcn_fractf(br * l);
      return cf{e * __builtin_amdgcn_cosf(ph), e * __builtin_amdgcn_sinf(ph)};
    };
    char* trow = smem + 0 + mode * 4;
    auto put = [&](int row, float re, float im) {
      typedef __attribute__((ext_vector_type(2))) _Float16 f16x2;
      *(f16x2*)(trow + row * KG_RS) = __builtin_convertvector(f32x2{re, im}, f16x2);
    };
    const cf w128 = cexp(128.f);
    if (wv < 2) {
      const cf w1_ = cexp(1.f), w8 = cexp(8.f), w64 = cexp(64.f);
      cf e0 = {1.f, 0.f}, e1 = w64, x = cb;
#pragma unroll
      for (int j = 0; j < 8; ++j) {
        put(j, e0.re, -e0.im); put(8 + j, e1.re, -e1.im);
        const cf y = cmul(x, w128);
        put(16 + j, x.re, x.im); put(24 + j, y.re, y.im);
        if (j < 7) { e0 = cmul(e0, w8); e1 = cmul(e1, w8); x = cmul(x, w1_); }
      }
    } else {
      const cf w2_ = cexp(2.f), w4 = cmul(w2_, w2_);
      cf x = cmul(cb, cexp(256.f));
#pragma unroll
      for (int j = 0; j < 4; ++j) {
        const cf y = cmul(x, w128);
        put(32 + j, x.re, x.im); put(36 + j, y.re, y.im);
        if (j < 3) x = cmul(x, w2_);
      }
      x = cmul(cb, cexp(512.f));
#pragma unroll
      for (int j = 0; j < 4; ++j) {
        const cf y = cmul(x, w4);
        put(40 + 2 * j, x.re, x.im); put(41 + 2 * j, y.re, y.im);
        if (j < 3) x = cmul(x, w128);
      }
      x = cmul(cb, cexp(1024.f));
      cf z = cmul(cb, cexp(1536.f));
#pragma unroll
      for (int j = 0; j < 5; ++j) {
        if (j < 4) put(48 + j, x.re, x.im);
        put(52 + j, z.re, z.im);
        if (j < 4) { x = cmul(x, w128); z = cmul(z, w128); }
      }
    }
  }
  lds_barrier();
  if (wv < 3) {
    const int arow = 16 + 16 * wv + (wv == 2 && p > 8 ? 8 : p);
    f16x8 fa[8], fb[8];
#pragma unroll
    for (int k = 0; k < 8; ++k) {
      const char* q = smem + 0 + (16 * k + 4 * g) * 4;
      fa[k] = *(const f16x8*)(q + arow * KG_RS); fb[k] = *(const f16x8*)(q + p * KG_RS);
    }
    const f32x4 sc4 = *(const f32x4*)scl;
    f32x4 acc[4] = {};
    asm volatile("" : "+v"(acc[0]), "+v"(acc[1]), "+v"(acc[2]), "+v"(acc[3]));
    __builtin_amdgcn_sched_barrier(0);
#pragma unroll
    for (int k = 0; k < 8; ++k) acc[k >> 1] = __builtin_amdgcn_mfma_f32_16x16x32_f16(fa[k], fb[k], acc[k >> 1], 0, 0, 0);
    __builtin_amdgcn_sched_barrier(0);
    const f32x4 sum = (acc[0] * sc4[0] + acc[1] * sc4[1]) + (acc[2] * sc4[2] + acc[3] * sc4[3]);
    const int b0 = wv == 0 ? 4 * (g & 1) + 128 * (g >> 1) + 8 * p : (wv == 2 ? 512 + 64 * g + p : (g < 2 ? 256 + 64 * g + 4 * p : 256 + 64 * g + 2 * p));
    const int s1 = wv == 2 ? 16 : 1, s2 = (wv == 2 || (wv == 1 && g >= 2)) ? 32 : 2;
#pragma unroll
    for (int r = 0; r < 4; ++r) ktab[min(b0 + (r & 1) * s1 + (r >> 1) * s2, 656)] = sum[r];
  }
}

constexpr int RT0 = 192;
constexpr int K0_LDS = 57 * KG_RS + 16;
__global__ __launch_bounds__(256, 5) void k0_prep(const u16* x, const u16* Are, const u16* Aim, const u16* Bre, const u16* Bim,
    const u16* Cre, const u16* Cim, const u16* logdt, const u16* w1, const u16* w3, const u16* w2, const u16* c, const u16* wmod, char* ws) {
  extern __shared__ __attribute__((aligned(16))) char smem[];
  KT_BEGIN(1)
  const int b = blockIdx.x < 920 ? (blockIdx.x < 576 ? blockIdx.x + 344 : 919 - blockIdx.x) : blockIdx.x;
  if (b < 216) {
    mod_block(b, c, wmod, (float*)(ws + WS_MODP), smem);
  } else if (b < 344) {
    ln1_block(b - 216, x, (u16*)(ws + WS_XT), (float*)(ws + WS_ZT), smem);
    if (b == 216) *(u32x4*)(ws + WS_CNT + threadIdx.x * 16) = u32x4{0, 0, 0, 0};
  } else if (b < 344 + 576) {
    kgen_block(b - 344, Are, Aim, Bre, Bim, Cre, Cim, logdt, (float*)(ws + WS_KR) + (b - 344) * 768, smem);
  } else {
    const int u = b - 920;
    u32x4 r0[3];
    if (u < 144) retile1_load(u, w1, w3, r0); else retile2_load(u, w2, r0);
    if (u < 144) retile_stage<2>(r0, smem); else retile_stage<3>(r0, smem);
    __syncthreads();
    if (u < 144) retile1_emit(u, smem, ws + WS_WT); else retile2_emit(u, smem, ws + WS_WT);
  }
  KT_END(1, ws);
}

constexpr int CP_STRIDE = 4160;
constexpr int K1_U = 0;
constexpr int K1_TAB = 4608;
constexpr int K1_OUT = K1_TAB + 4 * CP_STRIDE;
constexpr int K1_ST = 4 * 2560 * 4, K1_LDS = K1_ST + 12288;
__global__ __launch_bounds__(256, 3) void k1_conv(const u16* Dskip, const u16* w1, const u16* w3, const u16* w2, char* ws) {
  extern __shared__ __attribute__((aligned(16))) char smem[];
  STAMP_DECL
  STAMP(0);
  const int d = blockIdx.x, tid = threadIdx.x, lane = tid & 63, wv = __builtin_amdgcn_readfirstlane(tid >> 6);
  const int p = lane & 15, g = lane >> 4;
  float* tab = (float*)(smem + K1_OUT);
  const float* ktab = (const float*)(ws + WS_KR) + d * 768 + tid;
  const float k0 = ktab[0], k1 = ktab[256], k2 = ktab[tid <= 128 ? 512 : 0];
  const u32x4 xv = *(const u32x4*)((const u16*)(ws + WS_XT) + (size_t)d * L + 8 * tid);
  const float* stat = (const float*)(ws + WS_ZT) + 8 * tid;
  const f32x4 m0 = *(const f32x4*)stat, m1 = *(const f32x4*)(stat + 4), r0 = *(const f32x4*)(stat + L), r1 = *(const f32x4*)(stat + L + 4);
  float mq = ((const float*)(ws + WS_MODP))[(lane & 7) * 3456 + min(lane >> 3, 5) * D + d];
  const float dsk = bf2f(Dskip[d]);
  __builtin_amdgcn_sched_barrier(0);
  tab[tid] = k0; tab[256 + tid] = k1;
  if (tid <= 128) tab[512 + tid] = k2;
  STAMP(1); STAMP(2);
  mq += __int_as_float(__builtin_amdgcn_update_dpp(0, __float_as_int(mq), 0xB1, 0xf, 0xf, false));
  mq += __int_as_float(__builtin_amdgcn_update_dpp(0, __float_as_int(mq), 0x4E, 0xf, 0xf, false));
  mq += __int_as_float(__builtin_amdgcn_update_dpp(0, __float_as_int(mq), 0x141, 0xf, 0xf, false));
  mq = rb(mq);
  auto bc = [&](int i) { return __int_as_float(__builtin_amdgcn_readlane(__float_as_int(mq), 8 * i)); };
  const float sa = bc(0), ca1 = rb(1.f + bc(1)), ga = bc(2);
  u32x4 hp;
#pragma unroll
  for (int e = 0; e < 4; ++e) {
    const f32x2 mm = e < 2 ? f32x2{m0[2 * e], m0[2 * e + 1]} : f32x2{m1[2 * e - 4], m1[2 * e - 3]};
    const f32x2 rr = e < 2 ? f32x2{r0[2 * e], r0[2 * e + 1]} : f32x2{r1[2 * e - 4], r1[2 * e - 3]};
    const f32x2 hh = __builtin_elementwise_fma((f32x2{bflo(xv[e]), bfhi(xv[e])} - mm) * rr, f32x2{ca1, ca1}, f32x2{sa, sa});
    hp[e] = pk2(hh.x, hh.y);
  }
  *(u32x4*)(smem + K1_U + 480 + 16 * tid) = hp;
  if (tid < 30) *(u32x4*)(smem + K1_U + 16 * tid) = u32x4{0, 0, 0, 0};
  STAMP(3);
  lds_barrier();
  u32x4 rt[3];
  const bool side = (d & 255) >= 64;
  const int ru = RT0 + (d & 255) - 64 + 192 * (d >> 8);
  if (side) retile2_load(ru, w2, rt);
  unsigned pf = 0;
  if (side && wv == 3) pf = *(const unsigned*)(((d >> 8) ? w3 : w1) + (size_t)((d & 255) - 64) * F + 576 * (lane >> 3) + 64 * (lane & 7));
  __builtin_amdgcn_sched_barrier(0);
  {
    float kv[12];
#pragma unroll
    for (int i = 0; i < 12; i += 2) {
      const int l = 2047 - (8 * tid + i);
      const unsigned bits = pk2((float)l, (float)(l - 1));
      const int i0 = l < 128 ? (l < 0 ? 0 : l) : (int)(bits & 0xffff) - 17024, i1 = l - 1 < 128 ? (l < 1 ? 0 : l - 1) : (int)(bits >> 16) - 17024;
      kv[i] = tab[i0]; kv[i + 1] = tab[i1];
      if (i >= 8 && tid == 255) kv[i] = kv[i + 1] = 0.f;
    }
    unsigned w[6];
#pragma unroll
    for (int i = 0; i < 6; ++i) w[i] = pk2(kv[2 * i], kv[2 * i + 1]);
    auto al = [](unsigned hi, unsigned lo) { return __builtin_amdgcn_alignbit(hi, lo, 16); };
    char* cp = smem + K1_TAB + 16 * tid;
    *(u32x4*)(cp + 0 * CP_STRIDE) = u32x4{w[0], w[1], w[2], w[3]};
    *(u32x4*)(cp + 1 * CP_STRIDE) = u32x4{al(w[1], w[0]), al(w[2], w[1]), al(w[3], w[2]), al(w[4], w[3])};
    *(u32x4*)(cp + 2 * CP_STRIDE) = u32x4{w[1], w[2], w[3], w[4]};
    *(u32x4*)(cp + 3 * CP_STRIDE) = u32x4{al(w[2], w[1]), al(w[3], w[2]), al(w[4], w[3]), al(w[5], w[4])};
    if (tid < 8) *(u32x4*)(smem + K1_TAB + (tid >> 1) * CP_STRIDE + 4096 + 16 * (tid & 1)) = u32x4{0, 0, 0, 0};
  }
  STAMP(4);
  lds_barrier();
  const int sg = (3 - p) & 3;
  const unsigned abase = K1_TAB + sg * CP_STRIDE + 2 * (2031 + 8 * g - p - sg);
  const unsigned bbase = K1_U + 2 * (240 + 16 * p - 16 + 8 * g);
  f32x4 acc[8] = {};
#pragma unroll
  for (int i = 0; i < 8; ++i) asm volatile("" : "+v"(acc[i]));
#pragma unroll
  for (int cc = 0; cc < 2; ++cc) {
    const int c0 = 2 * wv + cc;
    bf16x8 A[8], B[8];
#pragma unroll
    for (int i = 0; i < 8; ++i) {
      const char* pa = smem + abase - 64 * c0 - 512 * i;
      u32x2 lo = *(const u32x2*)pa, hi = *(const u32x2*)(pa + 8);
      A[i] = __builtin_bit_cast(bf16x8, u32x4{lo.x, lo.y, hi.x, hi.y});
      B[i] = *(const bf16x8*)(smem + bbase - 64 * c0 + 512 * i);
    }
#pragma unroll
    for (int c1 = 0; c1 < 8; ++c1)
#pragma unroll
      for (int i = c1; i < 8; ++i) acc[i] = __builtin_amdgcn_mfma_f32_16x16x32_bf16(A[c1], B[i - c1], acc[i], 0, 0, 0);
  }
  float* red = (float*)smem;
  if (side) retile_stage<3>(rt, smem + K1_ST);
  STAMP(5);
  lds_barrier();
#pragma unroll
  for (int i = 0; i < 8; ++i) *(f32x4*)(red + wv * 2560 + (16 * i + p) * 20 + 4 * g) = acc[i];
  lds_barrier();
  STAMP(6);
  float y[8];
  {
    const int o = (tid >> 1) * 20 + (tid & 1) * 8;
#pragma unroll
    for (int hh = 0; hh < 2; ++hh) {
      f32x4 a = *(const f32x4*)(red + o + 4 * hh), b = *(const f32x4*)(red + 2560 + o + 4 * hh);
      f32x4 c = *(const f32x4*)(red + 5120 + o + 4 * hh), e = *(const f32x4*)(red + 7680 + o + 4 * hh);
      f32x4 s = (a + b) + (c + e);
#pragma unroll
      for (int k = 0; k < 4; ++k) y[4 * hh + k] = s[k];
    }
  }
  float o[8];
#pragma unroll
  for (int e = 0; e < 8; ++e) o[e] = fmaf(ga, fmaf(elem(hp, e), dsk, y[e]), elem(xv, e));
  *(u32x4*)((u16*)(ws + WS_X1T) + (size_t)d * L + 8 * tid) = u32x4{pk2(o[0], o[1]), pk2(o[2], o[3]), pk2(o[4], o[5]), pk2(o[6], o[7])};
  if (side) retile2_emit(ru, smem + K1_ST, ws + WS_WT);
  if (tid >= 24 && tid < 48 && (tid & 7) == 0) ((u16*)(ws + WS_MODF))[((tid >> 3) - 3) * D + d] = (u16)pk2(tid == 32 ? 1.f + mq : mq, 0.f);
  asm volatile("" :: "v"(pf));
  STAMP(7);
}

constexpr int K3_H = 73728, K3_ST = 2 * 73728, K3_LDS = 2 * 73728 + 8192;
#define LDIDX(i) (i)
constexpr int RING = 16;
DEV unsigned swiglu_tile(const f32x4& c) {
  auto s0 = __builtin_amdgcn_permlane32_swap(__float_as_uint(c[0]), __float_as_uint(c[2]), false, false);
  auto s1 = __builtin_amdgcn_permlane32_swap(__float_as_uint(c[1]), __float_as_uint(c[3]), false, false);
  const f32x2 gt = {__uint_as_float(s0[0]), __uint_as_float(s1[0])}, up = {__uint_as_float(s0[1]), __uint_as_float(s1[1])};
  const f32x2 ar = gt * -1.44269504088896341f;
  const f32x2 dn = f32x2{__builtin_amdgcn_exp2f(ar.x), __builtin_amdgcn_exp2f(ar.y)} + 1.f;
  const f32x2 hv = gt * f32x2{__builtin_amdgcn_rcpf(dn.x), __builtin_amdgcn_rcpf(dn.y)} * up;
  return pk2(hv.x, hv.y);
}
DEV bf16x8 tfrag(const u32x4 (&raw)[8], int i) {
  const unsigned sel = (i & 1) ? 0x07060302u : 0x05040100u;
  u32x4 o;
#pragma unroll
  for (int q = 0; q < 4; ++q) o[q] = __builtin_amdgcn_perm(raw[2 * q + 1][i >> 1], raw[2 * q][i >> 1], sel);
  return __builtin_bit_cast(bf16x8, o);
}
template <int NT>
DEV void ffn_phase2(__amdgpu_buffer_rsrc_t rs, int wv, int lane, char* smem, int s, int mt, u16* out, bf16x8 (&ring)[RING], char* ws STAMP_ARG) {
  const unsigned vo = lane * 16;
  constexpr int F0 = NT == 5 ? 36 : 0, NFR = F0 + 18 * NT;
  auto ldw = [&](int idx) { return __builtin_bit_cast(bf16x8, __builtin_amdgcn_raw_buffer_load_b128(rs, vo, idx * 1024, 0)); };
  bf16x8 b[2][4];
  f32x4 oc[NT][4];
#pragma unroll
  for (int tt = 0; tt < 4; ++tt) b[0][tt] = *(const bf16x8*)(smem + K3_H + tt * 1024 + lane * 16);
#pragma unroll
  for (int ks = 0; ks < 18; ++ks) {
    if (ks < 17) {
#pragma unroll
      for (int tt = 0; tt < 4; ++tt) b[(ks + 1) & 1][tt] = *(const bf16x8*)(smem + K3_H + (ks + 1) * 4096 + tt * 1024 + lane * 16);
    }
#pragma unroll
    for (int ti = 0; ti < NT; ++ti) {
      const int i = F0 + ks * NT + ti;
#pragma unroll
      for (int tt = 0; tt < 4; ++tt)
        oc[ti][tt] = __builtin_amdgcn_mfma_f32_16x16x32_bf16(ring[i % RING], b[ks & 1][tt], ks == 0 ? f32x4{0.f, 0.f, 0.f, 0.f} : oc[ti][tt], 0, 0, 0);
      if (i + RING < NFR) ring[i % RING] = ldw(i + RING);
      __builtin_amdgcn_sched_barrier(0);
    }
  }
  STAMP(8);
  const int dt0 = NT == 5 ? 5 * wv : 20 + 4 * (wv - 4);
  char* pbase = ws + WS_P + (size_t)mt * 8 * 73728;
  __amdgpu_buffer_rsrc_t rp = __builtin_amdgcn_make_buffer_rsrc(pbase, 0, 8 * 73728, 0x00020000);
#pragma unroll
  for (int ti = 0; ti < NT; ++ti)
#pragma unroll
    for (int tp = 0; tp < 2; ++tp) {
      const f32x4 u = oc[ti][2 * tp], v = oc[ti][2 * tp + 1];
      __builtin_amdgcn_raw_buffer_store_b128(u32x4{pk2(u[0], u[1]), pk2(u[2], u[3]), pk2(v[0], v[1]), pk2(v[2], v[3])}, rp,
                                             vo + (s * 72 + dt0 * 2) * 1024 + (ti * 2 + tp) * 1024, 0, 16  );
    }
  const u16* x1T = (const u16*)(ws + WS_X1T);
  const u16* mf = (const u16*)(ws + WS_MODF);
  constexpr int NU = NT == 5 ? 1 : 2;
  const int nu = wv == 7 ? 2 : 1;
  unsigned xr[NU][8], gb[NU][2];
#pragma unroll
  for (int k = 0; k < NU; ++k)
    if (k < nu) {
      const int un = 9 * s + wv + k, d0 = 16 * (un >> 1) + 4 * (lane >> 4), t0 = 64 * mt + 32 * (un & 1) + (lane & 15);
#pragma unroll
      for (int e = 0; e < 4; ++e) { xr[k][e] = x1T[(size_t)(d0 + e) * L + t0]; xr[k][4 + e] = x1T[(size_t)(d0 + e) * L + t0 + 16]; }
      const u32x2 gg = *(const u32x2*)(mf + 2 * D + d0);
      gb[k][0] = gg.x; gb[k][1] = gg.y;
    }
  asm volatile("s_waitcnt vmcnt(0)" ::: "memory");
  STAMP(9);
  __builtin_amdgcn_s_barrier();
  {
    const char* cnt = ws + WS_CNT + mt * 128;
    unsigned zero = 0, one = 1, tmp, sv, left;
    unsigned long long ex;
    asm volatile(
        "s_cmp_lg_u32 %[wv], 0\n\t"
        "s_cbranch_scc1 2f\n\t"
        "s_mov_b64 %[ex], exec\n\t"
        "s_mov_b64 exec, 1\n\t"
        "s_nop 1\n\t"
        "global_atomic_add %[zero], %[one], %[cnt]\n\t"
        "s_mov_b32 %[left], 0x100000\n\t"
        "1:\n\t"
        "global_load_dword %[tmp], %[zero], %[cnt] sc1\n\t"
        "s_waitcnt vmcnt(0)\n\t"
        "v_readfirstlane_b32 %[sv], %[tmp]\n\t"
        "s_cmp_ge_u32 %[sv], 8\n\t"
        "s_cbranch_scc1 3f\n\t"
        "s_sleep 2\n\t"
        "s_sub_u32 %[left], %[left], 1\n\t"
        "s_cmp_lg_u32 %[left], 0\n\t"
        "s_cbranch_scc1 1b\n\t"
        "3:\n\t"
        "s_mov_b64 exec, %[ex]\n\t"
        "2:\n\t"
        : [tmp] "=&v"(tmp), [sv] "=&s"(sv), [left] "=&s"(left), [ex] "=&s"(ex)
        : [wv] "s"(wv), [zero] "v"(zero), [one] "v"(one), [cnt] "s"(cnt)
        : "memory", "scc");
  }
  asm volatile("" ::: "memory");
  __builtin_amdgcn_s_barrier();
  asm volatile("" ::: "memory");
  STAMP(10);
  u32x4 pv[NU][8];
#pragma unroll
  for (int k = 0; k < NU; ++k)
    if (k < nu)
#pragma unroll
      for (int q = 0; q < 8; ++q) pv[k][q] = __builtin_amdgcn_raw_buffer_load_b128(rp, vo, q * 73728 + (9 * s + wv + k) * 1024, 16  );
#pragma unroll
  for (int k = 0; k < NU; ++k)
    if (k < nu) {
      const int un = 9 * s + wv + k, d0 = 16 * (un >> 1) + 4 * (lane >> 4), t0 = 64 * mt + 32 * (un & 1) + (lane & 15);
      float o[8];
#pragma unroll
      for (int e = 0; e < 8; ++e) {
        const float a = ((elem(pv[k][0], e) + elem(pv[k][1], e)) + (elem(pv[k][2], e) + elem(pv[k][3], e))) +
                        ((elem(pv[k][4], e) + elem(pv[k][5], e)) + (elem(pv[k][6], e) + elem(pv[k][7], e)));
        const unsigned gw = gb[k][(e & 3) >> 1];
        o[e] = bf2f(xr[k][e]) + rb(((e & 1) ? bfhi(gw) : bflo(gw)) * rb(a));
      }
      *(u32x2*)(out + (size_t)t0 * D + d0) = u32x2{pk2(o[0], o[1]), pk2(o[2], o[3])};
      *(u32x2*)(out + (size_t)(t0 + 16) * D + d0) = u32x2{pk2(o[4], o[5]), pk2(o[6], o[7])};
    }
  STAMP(11);
  STAMP_OUT(ws, 12);
}

DEV void ffn_phase1(const char* wp, const char* whead, const char* pfp, __amdgpu_buffer_rsrc_t rs, int wv, int lane, char* smem, u32x4 (&raw)[2][8], int s, int mt, u16* out, char* ws STAMP_ARG) {
  bf16x8 ring[RING];
  const unsigned vo = lane * 16;
  auto ldw = [&](int idx) { return __builtin_bit_cast(bf16x8, __builtin_amdgcn_raw_buffer_load_b128(rs, vo, idx * 1024, 0)); };
  unsigned boff[4];
#pragma unroll
  for (int tt = 0; tt < 4; ++tt) boff[tt] = tt * 1024 + ((lane ^ ((2 * tt + ((lane >> 3) & 1)) & 7)) * 16);
  f32x4 acc[8][4] = {};
  bf16x8 b[4];
#pragma unroll
  for (int tt = 0; tt < 4; ++tt) b[tt] = *(const bf16x8*)(smem + boff[tt]);
  bf16x8 a = tfrag(raw[0], 0);
  auto ldrow = [&](int j) {
    const char* p;
    asm("v_lshl_add_u64 %0, %1, 0, %2" : "=v"(p) : "v"(wp), "s"((unsigned long long)j * (F * 2)));
    return *(const __attribute__((address_space(1))) u32x4*)p;
  };
#define SGB(m, n) __builtin_amdgcn_sched_group_barrier(m, n, 0)
  auto step = [&](int par, int ksn, unsigned inc) {
    bf16x8 hi[4];
#pragma unroll
    for (int i = 0; i < 4; ++i) {
      bf16x8 an;
      if (i < 3) an = tfrag(raw[par], i + 1);
      hi[i] = tfrag(raw[par], 4 + i);
#pragma unroll
      for (int tt = 0; tt < 4; ++tt) acc[i][tt] = __builtin_amdgcn_mfma_f32_16x16x32_bf16(a, b[tt], acc[i][tt], 0, 0, 0);
      if (i < 3) a = an;
      SGB(0x8, 1); SGB(0x2, 2); SGB(0x8, 1); SGB(0x2, 2); SGB(0x8, 1); SGB(0x2, 2); SGB(0x8, 1); SGB(0x2, 2);
      __builtin_amdgcn_sched_barrier(0);
    }
#pragma unroll
    for (int i = 4; i < 6; ++i) {
#pragma unroll
      for (int j = 0; j < 3; ++j) raw[par][3 * (i - 4) + j] = ldrow(3 * (i - 4) + j);
#pragma unroll
      for (int tt = 0; tt < 4; ++tt) acc[i][tt] = __builtin_amdgcn_mfma_f32_16x16x32_bf16(hi[i - 4], b[tt], acc[i][tt], 0, 0, 0);
      SGB(0x8, 1); SGB(0x2, 1); SGB(0x20, 1); SGB(0x8, 1); SGB(0x2, 1); SGB(0x20, 1); SGB(0x8, 1); SGB(0x2, 1); SGB(0x20, 1); SGB(0x8, 1);
      __builtin_amdgcn_sched_barrier(0);
    }
    const char* bn = smem + ksn * 4096;
#pragma unroll
    for (int tt = 0; tt < 4; ++tt) {
      if (tt < 2) raw[par][6 + tt] = ldrow(6 + tt);
      if (tt == 2) { a = tfrag(raw[par ^ 1], 0); wp += inc; }
      acc[6][tt] = __builtin_amdgcn_mfma_f32_16x16x32_bf16(hi[2], b[tt], acc[6][tt], 0, 0, 0);
      acc[7][tt] = __builtin_amdgcn_mfma_f32_16x16x32_bf16(hi[3], b[tt], acc[7][tt], 0, 0, 0);
      b[tt] = *(const bf16x8*)(bn + boff[tt]);
      SGB(0x8, 1); SGB(0x2, 3); SGB(0x20, 1); SGB(0x8, 1); SGB(0x2, 3); SGB(0x100, 1);
      __builtin_amdgcn_sched_barrier(0);
    }
  };
  unsigned pf = 0;
#pragma unroll 1
  for (int it = 0; it < 9; ++it) {
    asm volatile("" :: "v"(pf));
    if (it < 6 && wv == it) pf = *(const unsigned*)(pfp + (size_t)it * 64 * (F * 2));
    const bool last = it == 8;
    if (last) wp = whead;
    const unsigned inc = (last ? 8 : 32) * (F * 2);
    step(0, 2 * it + 1, inc);
    step(1, last ? 0 : 2 * it + 2, inc);
  }
#pragma unroll
  for (int i = 0; i < 16; ++i) ring[i] = __builtin_bit_cast(bf16x8, raw[i >> 3][i & 7]);
  STAMP(5);
  char* hw0 = smem + K3_H + (2 * wv) * 4096 + lane * 16;
  auto put4 = [&](int h, int tt) {
    *(u32x4*)(hw0 + h * 4096 + tt * 1024) = u32x4{swiglu_tile(acc[4 * h][tt]), swiglu_tile(acc[4 * h + 1][tt]), swiglu_tile(acc[4 * h + 2][tt]), swiglu_tile(acc[4 * h + 3][tt])};
  };
  if (wv < 4) {
    put4(0, 0); put4(0, 1);
    __builtin_amdgcn_sched_barrier(0);
    f32x4 am[2][4] = {};
#pragma unroll
    for (int ks = 0; ks < 18; ++ks) {
#pragma unroll
      for (int tt = 0; tt < 4; ++tt) {
        am[0][tt] = __builtin_amdgcn_mfma_f32_16x16x32_bf16(ring[(2 * ks) % RING], b[tt], am[0][tt], 0, 0, 0);
        am[1][tt] = __builtin_amdgcn_mfma_f32_16x16x32_bf16(ring[(2 * ks + 1) % RING], b[tt], am[1][tt], 0, 0, 0);
        if (ks < 17) b[tt] = *(const bf16x8*)(smem + (ks + 1) * 4096 + boff[tt]);
      }
      ring[(2 * ks) % RING] = ldw(2 * ks + RING);
      ring[(2 * ks + 1) % RING] = ldw(2 * ks + 1 + RING);
      if (ks < 18 && ks % 3 == 0) put4((ks / 3 + 2) >> 2, (ks / 3 + 2) & 3);
      __builtin_amdgcn_sched_barrier(0);
    }
    char* hw8 = smem + K3_H + (16 + (wv >> 1)) * 4096 + lane * 16 + (wv & 1) * 8;
#pragma unroll
    for (int tt = 0; tt < 4; ++tt) *(u32x2*)(hw8 + tt * 1024) = u32x2{swiglu_tile(am[0][tt]), swiglu_tile(am[1][tt])};
    STAMP(6);
    __syncthreads();
    STAMP(7);
    ffn_phase2<5>(rs, wv, lane, smem, s, mt, out, ring, ws STAMP_PASS);
    __builtin_amdgcn_endpgm();
  }
#pragma unroll
  for (int q = 0; q < 8; ++q) put4(q >> 2, q & 3);
  STAMP(6);
  __syncthreads();
  STAMP(7);
  ffn_phase2<4>(rs, wv, lane, smem, s, mt, out, ring, ws STAMP_PASS);
  __builtin_amdgcn_endpgm();
}
__global__ __launch_bounds__(512, 2) void k3_ffn(const u16* w1, const u16* w3, char* ws, u16* out) {
  extern __shared__ __attribute__((aligned(16))) char smem[];
  const int tid = threadIdx.x, lane = tid & 63;
  const int wv = __builtin_amdgcn_readfirstlane(tid >> 6);
  STAMP_DECL
  STAMP(0);
  const int xc = blockIdx.x & 7, bi = blockIdx.x >> 3;
  const int s = (xc % (8 / SPX)) * SPX + bi % SPX, mt = (xc / (8 / SPX)) * (32 / SPX) + bi / SPX;
  const u16* x1T = (const u16*)(ws + WS_X1T) + 64 * mt;
  const u16* mf = (const u16*)(ws + WS_MODF);
  const int tc = tid & 7, cg = tid >> 3;
  u32x4 r[9];
#pragma unroll
  for (int e = 0; e < 8; ++e) r[e] = *(const u32x4*)(x1T + (size_t)(8 * cg + e) * L + 8 * tc);
  r[8] = *(const u32x4*)(x1T + (size_t)(512 + cg) * L + 8 * tc);
  const u32x4 sbv = *(const u32x4*)(mf + 8 * cg), cbv = *(const u32x4*)(mf + D + 8 * cg);
  const float sb8 = bf2f(mf[512 + cg]), cb8 = bf2f(mf[D + 512 + cg]);
  __builtin_amdgcn_sched_barrier(0);
  asm volatile("s_waitcnt vmcnt(0)" ::: "memory");
  STAMP(1);
  __builtin_amdgcn_sched_barrier(0);
  f32x2 xv[9][4];
#pragma unroll
  for (int e = 0; e < 9; ++e)
#pragma unroll
    for (int t = 0; t < 4; ++t) xv[e][t] = f32x2{bflo(r[e][t]), bfhi(r[e][t])};
  float s1, s2;
  {
    f32x2 p1[4], p2[4];
#pragma unroll
    for (int t = 0; t < 4; ++t) {
      p1[t] = xv[0][t]; p2[t] = xv[0][t] * xv[0][t];
#pragma unroll
      for (int e = 1; e < 9; ++e) { p1[t] += xv[e][t]; p2[t] += xv[e][t] * xv[e][t]; }
    }
    auto sw32 = [](float a, float b) { auto q = __builtin_amdgcn_permlane32_swap(__float_as_uint(a), __float_as_uint(b), false, false); return __uint_as_float(q[0]) + __uint_as_float(q[1]); };
    auto sw16 = [](float a, float b) { auto q = __builtin_amdgcn_permlane16_swap(__float_as_uint(a), __float_as_uint(b), false, false); return __uint_as_float(q[0]) + __uint_as_float(q[1]); };
    float u1[4], u2[4];
#pragma unroll
    for (int t = 0; t < 4; ++t) { u1[t] = sw32(p1[t >> 1][t & 1], p1[2 + (t >> 1)][t & 1]); u2[t] = sw32(p2[t >> 1][t & 1], p2[2 + (t >> 1)][t & 1]); }
    const float v10 = sw16(u1[0], u1[2]), v11 = sw16(u1[1], u1[3]), v20 = sw16(u2[0], u2[2]), v21 = sw16(u2[1], u2[3]);
    const bool b3 = lane & 8;
    auto ror8 = [](float v) { return __int_as_float(__builtin_amdgcn_update_dpp(0, __float_as_int(v), 0x128, 0xf, 0xf, false)); };
    s1 = (b3 ? v11 : v10) + ror8(b3 ? v10 : v11);
    s2 = (b3 ? v21 : v20) + ror8(b3 ? v20 : v21);
  }
  float* st = (float*)(smem + K3_ST);
  *(f32x2*)(st + (wv * 64 + 8 * tc + (lane >> 3)) * 2) = f32x2{s1, s2};
  __builtin_amdgcn_sched_barrier(0);
  char* wbase = ws + WS_WT + ((size_t)s * FR_S + wave_frag_off(wv)) * 1024;
  __amdgpu_buffer_rsrc_t rs = __builtin_amdgcn_make_buffer_rsrc(wbase, 0, 126 * 1024, 0x00020000);
  const char* wp = (const char*)((lane & 8) ? w3 : w1) + ((size_t)(8 * (lane >> 4)) * F + 576 * s + 64 * wv + 8 * (lane & 7)) * 2;
  u32x4 raw[2][8];
#pragma unroll
  for (int i = 0; i < 2; ++i) {
#pragma unroll
    for (int j = 0; j < 8; ++j) raw[i][j] = *(const u32x4*)(wp + (size_t)j * (F * 2));
    wp += (size_t)32 * (F * 2);
  }
  __builtin_amdgcn_sched_barrier(0);
  STAMP(2);
  asm volatile("s_waitcnt lgkmcnt(0)" ::: "memory");
  __builtin_amdgcn_s_barrier();
  STAMP(3);
  float* sw = (float*)(smem + K3_ST + 4096) + wv * 128;
  {
    float a = 0.f, b = 0.f;
#pragma unroll
    for (int w = 0; w < 8; ++w) { f32x2 v = *(const f32x2*)(st + (w * 64 + lane) * 2); a += v.x; b += v.y; }
    const float mean = a * (1.f / D);
    const float var = fmaxf(b * (1.f / D) - mean * mean, 0.f);
    const float rd = 1.f / rb(sqrtf(rb(rb(var) + 1e-5f)));
    sw[lane] = rd; sw[64 + lane] = -rb(mean) * rd;
  }
  const f32x4 rd0 = *(const f32x4*)(sw + 8 * tc), rd1 = *(const f32x4*)(sw + 8 * tc + 4);
  const f32x4 nm0 = *(const f32x4*)(sw + 64 + 8 * tc), nm1 = *(const f32x4*)(sw + 64 + 8 * tc + 4);
  const f32x2 rdp[4] = {{rd0[0], rd0[1]}, {rd0[2], rd0[3]}, {rd1[0], rd1[1]}, {rd1[2], rd1[3]}};
  const f32x2 nmp[4] = {{nm0[0], nm0[1]}, {nm0[2], nm0[3]}, {nm1[0], nm1[1]}, {nm1[2], nm1[3]}};
  char* base = smem + (cg >> 2) * 4096 + (tc >> 1) * 1024 + (cg & 3) * 256 + (tc & 1) * 128;
  char* base8 = smem + (16 + (wv >> 2)) * 4096 + (tc >> 1) * 1024 + (wv & 3) * 256 + (tc & 1) * 128 + (cg & 7) * 2;
#pragma unroll
  for (int t = 0; t < 4; ++t) {
    f32x2 hv[9];
#pragma unroll
    for (int e = 0; e < 9; ++e) {
      const float cb = e < 8 ? elem(cbv, e) : cb8, sb = e < 8 ? elem(sbv, e) : sb8;
      hv[e] = __builtin_elementwise_fma(__builtin_elementwise_fma(xv[e][t], rdp[t], nmp[t]), f32x2{cb, cb}, f32x2{sb, sb});
    }
#pragma unroll
    for (int p = 0; p < 2; ++p) {
      *(u32x4*)(base + (((2 * t + p) ^ tc) * 16)) = u32x4{pk2(hv[0][p], hv[1][p]), pk2(hv[2][p], hv[3][p]), pk2(hv[4][p], hv[5][p]), pk2(hv[6][p], hv[7][p])};
      *(u16*)(base8 + (((2 * t + p) ^ tc) * 16)) = (u16)pk2(hv[8][p], 0.f);
    }
  }
  asm volatile("s_waitcnt lgkmcnt(0)" ::: "memory");
  __builtin_amdgcn_s_barrier();
  STAMP(4);
  const char* pfp = (const char*)((lane & 8) ? w3 : w1) + ((size_t)(192 + 4 * (bi / SPX) + (lane >> 4)) * F + 576 * s + 64 * (lane & 7)) * 2;
  ffn_phase1(wp, ws + WS_WT + ((size_t)s * FR_S + wv) * 1024 + lane * 16, pfp, rs, wv, lane, smem, raw, s, mt, out, ws STAMP_PASS);
}

extern "C" void kernel_launch(void* const* d_in, const int* in_sizes, int n_in, void* d_out, int out_size, void* d_ws,
                              size_t ws_size, hipStream_t stream) {
  const u16* x = (const u16*)d_in[0];
  const u16 *Are = (const u16*)d_in[1], *Aim = (const u16*)d_in[2], *Bre = (const u16*)d_in[3], *Bim = (const u16*)d_in[4];
  const u16 *Cre = (const u16*)d_in[5], *Cim = (const u16*)d_in[6], *logdt = (const u16*)d_in[7], *Dskip = (const u16*)d_in[8];
  const u16 *w1 = (const u16*)d_in[10], *w3 = (const u16*)d_in[11], *w2 = (const u16*)d_in[12];
  const u16 *c = (const u16*)d_in[13], *wmod = (const u16*)d_in[14];
  char* ws = (char*)d_ws;
  (void)hipFuncSetAttribute((const void*)k3_ffn, hipFuncAttributeMaxDynamicSharedMemorySize, K3_LDS);
  k0_prep<<<920 + RT0, 256, K0_LDS, stream>>>(x, Are, Aim, Bre, Bim, Cre, Cim, logdt, w1, w3, w2, c, wmod, ws);
  k1_conv<<<D, 256, K1_LDS, stream>>>(Dskip, w1, w3, w2, ws);
  k3_ffn<<<256, 512, K3_LDS, stream>>>(w1, w3, ws, (u16*)d_out);
}
```

```cpp
#include <hip/hip_runtime.h>
#include <hip/hip_bf16.h>
#include <stdint.h>

typedef __attribute__((ext_vector_type(8))) __bf16 bf16x8;
typedef __attribute__((ext_vector_type(2))) __bf16 bf16x2;
typedef __attribute__((ext_vector_type(8))) _Float16 f16x8;
typedef __attribute__((ext_vector_type(4))) float f32x4;
typedef __attribute__((ext_vector_type(2))) float f32x2;
typedef __attribute__((ext_vector_type(4))) unsigned u32x4;
typedef __attribute__((ext_vector_type(2))) unsigned u32x2;
typedef __attribute__((ext_vector_type(4))) short s16x4;
typedef unsigned short u16;
#define LDSP __attribute__((address_space(3)))
#define DEV __device__ __forceinline__

#define STAMP(i)
#define STAMP_DECL
#define STAMP_ARG
#define STAMP_PASS
#define STAMP_OUT(ws, n)
#define KT_BEGIN(k)
#define KT_END(k, ws)
#define K2PRIO 1
#define PREF1 0
#define RLN2 0
#define RX1 1
#define RSW 0
#define LBAR 0
#define PREF 1
#define PREF2 0
#define SPX 2
constexpr int L = 2048, D = 576, NM = 128, F = 4608;
constexpr int KR_STRIDE = 2064;
constexpr int MODK = 8;
constexpr int FR_S = 144 + 664;

constexpr size_t WS_MODP = 0;
constexpr size_t WS_MODF = 114688;
constexpr size_t WS_CNT  = 118784;
constexpr size_t WS_XT   = 131072;
constexpr size_t WS_ZT   = WS_XT + (size_t)D * L * 2;
constexpr size_t WS_X1T  = WS_ZT + (size_t)D * L * 2;
constexpr size_t WS_KR   = WS_X1T + (size_t)D * L * 2;
constexpr size_t WS_WT   = (WS_KR + (size_t)D * KR_STRIDE * 2 + 4095) / 4096 * 4096;
constexpr size_t WS_P    = WS_WT + (size_t)8 * FR_S * 1024;

DEV float bf2f(unsigned v) { return __uint_as_float(v << 16); }
DEV float bflo(unsigned v) { return __uint_as_float(v << 16); }
DEV float bfhi(unsigned v) { return __uint_as_float(v & 0xffff0000u); }
DEV float rb(float x) { return (float)(__bf16)x; }
DEV f32x2 rb2(f32x2 x) { const f32x2 p = x * 65537.f; return __builtin_elementwise_fma(x, f32x2{-65536.f, -65536.f}, p); }
DEV unsigned pk2(float a, float b) { f32x2 v = {a, b}; bf16x2 r = __builtin_convertvector(v, bf16x2); return __builtin_bit_cast(unsigned, r); }
DEV float elem(const u32x4& v, int i) { unsigned w = v[i >> 1]; return (i & 1) ? bfhi(w) : bflo(w); }
DEV int wave_frag_off(int w) { return 144 - 16 + (w < 4 ? 110 * w : 440 + 56 * (w - 4)); }
DEV int frag_pos(int s, int w, int i) { return s * FR_S + (i < 16 ? i * 9 + w : wave_frag_off(w) + i); }

DEV void lds_barrier() { asm volatile("s_waitcnt lgkmcnt(0)" ::: "memory"); __builtin_amdgcn_s_barrier(); asm volatile("" ::: "memory"); }
DEV void retile1_load(int u, const u16* w1, const u16* w3, u32x4 (&r)[3]) {
  const int tid = threadIdx.x & 255, s = u & 7, ks = u >> 3;
  const size_t o = (size_t)(32 * ks + (tid >> 3)) * F + 576 * s + 512 + (tid & 7) * 8;
  r[0] = *(const u32x4*)(w1 + o);
  r[1] = *(const u32x4*)(w3 + o);
}
DEV void retile2_load(int u, const u16* w2, u32x4 (&r)[3]) {
  const int tid = threadIdx.x & 255, s = u & 7, v = (u >> 3) - 18, ks = (v * 43) >> 7, th = v - 3 * ks;
  const int kk = tid >> 3, gg = kk >> 3, jj = kk & 7, cp = 4 * (gg & 1) + 2 * (gg >> 1) + (jj & 1);
  const bool nrm = ks < 16;
  const int f = (nrm ? 64 * (ks >> 1) + 4 * (ks & 1) : 32 * ks) + (nrm ? 8 * cp + (jj >> 1) : cp + 8 * (jj >> 1));
  const u16* p = w2 + (size_t)(576 * s + f) * D + 192 * th + (tid & 7) * 8;
#pragma unroll
  for (int i = 0; i < 3; ++i) r[i] = *(const u32x4*)(p + 64 * i);
}
template <int N> DEV void retile_stage(const u32x4 (&r)[3], char* st) {
  const int tid = threadIdx.x & 255;
  if (N == 2) { *(u32x4*)(st + tid * 16) = r[0]; *(u32x4*)(st + 4096 + tid * 16) = r[1]; }
  else {
#pragma unroll
    for (int i = 0; i < 3; ++i) *(u32x4*)(st + (tid >> 3) * 384 + (tid & 7) * 16 + 128 * i) = r[i];
  }
}
template <int RS> DEV u32x4 tr_frag(const char* p) {
  s16x4 lo = __builtin_amdgcn_ds_read_tr16_b64_v4i16((LDSP s16x4*)p);
  s16x4 hi = __builtin_amdgcn_ds_read_tr16_b64_v4i16((LDSP s16x4*)(p + 4 * RS));
  u32x2 l2 = __builtin_bit_cast(u32x2, lo), h2 = __builtin_bit_cast(u32x2, hi);
  return u32x4{l2.x, l2.y, h2.x, h2.y};
}
DEV void retile1_emit(int u, const char* st, char* wt) {
  const int lane = threadIdx.x & 63, wv = __builtin_amdgcn_readfirstlane((threadIdx.x >> 6) & 3);
  const int s = u & 7, ks = u >> 3;
  const int g = lane >> 4, qq = (lane & 15) >> 2, pp = lane & 3;
  const char* p = st + (pp >> 1) * 4096 + (8 * g + qq) * 128 + (pp & 1) * 8 + wv * 32;
#pragma unroll
  for (int i = 0; i < 2; ++i)
    *(u32x4*)(wt + (size_t)frag_pos(s, wv, 2 * ks + i) * 1024 + lane * 16) = tr_frag<128>(p + i * 16);
}
DEV void retile2_emit(int u, const char* st, char* wt) {
  const int lane = threadIdx.x & 63, wv = __builtin_amdgcn_readfirstlane((threadIdx.x >> 6) & 3);
  const int s = u & 7, v = (u >> 3) - 18, ks = (v * 43) >> 7, th = v - 3 * ks;
  const int g = lane >> 4, qq = (lane & 15) >> 2, pp = lane & 3;
  const char* p = st + (8 * g + qq) * 384 + pp * 8 + wv * 96;
#pragma unroll
  for (int i = 0; i < 3; ++i) {
    const int dt = 12 * th + wv * 3 + i;
    const int wa = (dt * 13) >> 6, pa = 128 + 110 * wa + 36 + ks * 5 + (dt - 5 * wa);
    const int wb = (dt - 20) >> 2, ib = ks * 4 + ((dt - 20) & 3);
    const int pb = ib < 16 ? ib * 9 + 4 + wb : 128 + 440 + 56 * wb + ib;
    const int pos = __builtin_amdgcn_readfirstlane(s * FR_S + (dt < 20 ? pa : pb));
    *(u32x4*)(wt + (size_t)pos * 1024 + lane * 16) = tr_frag<384>(p + i * 32);
  }
}

#define RT_K1 144
struct cf { float re, im; };
DEV cf cmul(cf a, cf b) {
  cf r;
  asm("v_mul_f32 %0, %2, %4\n\tv_mul_f32 %1, %2, %5\n\tv_fma_f32 %0, -%3, %5, %0\n\tv_fma_f32 %1, %3, %4, %1"
      : "=&v"(r.re), "=&v"(r.im) : "v"(a.re), "v"(a.im), "v"(b.re), "v"(b.im));
  return r;
}
constexpr int LN_RS = 1152 + 16;
DEV void ln1_block(int u, const u16* x, u16* xT, float* stat, char* smem) {
  const int tid = threadIdx.x & 255, tok = tid >> 4, c16 = tid & 15, lane = tid & 63, wv = __builtin_amdgcn_readfirstlane(tid >> 6);
  const int t0 = 16 * ((u & 7) * 16 + (u >> 3));
  const u16* xr = x + (size_t)(t0 + tok) * D + c16 * 8;
  u32x4 r[5];
#pragma unroll
  for (int i = 0; i < 4; ++i) r[i] = *(const u32x4*)(xr + 128 * i);
  r[4] = c16 < 8 ? *(const u32x4*)(xr + 512) : u32x4{0, 0, 0, 0};
#pragma unroll
  for (int i = 0; i < 4; ++i) *(u32x4*)(smem + tok * LN_RS + (c16 + 16 * i) * 16) = r[i];
  if (c16 < 8) *(u32x4*)(smem + tok * LN_RS + (c16 + 64) * 16) = r[4];
  f32x2 sp = {0.f, 0.f};
#pragma unroll
  for (int i = 0; i < 5; ++i)
#pragma unroll
    for (int e = 0; e < 4; ++e) sp += f32x2{bflo(r[i][e]), bfhi(r[i][e])};
  auto sum16 = [](float v) {
    v += __int_as_float(__builtin_amdgcn_update_dpp(0, __float_as_int(v), 0xB1, 0xf, 0xf, false));
    v += __int_as_float(__builtin_amdgcn_update_dpp(0, __float_as_int(v), 0x4E, 0xf, 0xf, false));
    v += __int_as_float(__builtin_amdgcn_update_dpp(0, __float_as_int(v), 0x124, 0xf, 0xf, false));
    v += __int_as_float(__builtin_amdgcn_update_dpp(0, __float_as_int(v), 0x128, 0xf, 0xf, false));
    return v;
  };
  const float mean = sum16(sp.x + sp.y) * (1.f / D);
  f32x2 qp = {0.f, 0.f};
#pragma unroll
  for (int i = 0; i < 5; ++i)
#pragma unroll
    for (int e = 0; e < 4; ++e) {
      f32x2 dl = f32x2{bflo(r[i][e]), bfhi(r[i][e])} - mean;
      if (i == 4 && c16 >= 8) dl = f32x2{0.f, 0.f};
      qp += dl * dl;
    }
  const float q = sum16(qp.x + qp.y);
  if (c16 == 0) { stat[t0 + tok] = rb(mean); stat[L + t0 + tok] = 1.f / rb(sqrtf(rb(rb(q * (1.f / D)) + 1e-5f))); }
  __syncthreads();
  const int g = lane >> 4, qq = (lane & 15) >> 2, pp = lane & 3;
  const char* p = smem + (8 * (g & 1) + qq) * LN_RS + pp * 8 + (g >> 1) * 32 + wv * 64;
  u16* o = xT + (size_t)(32 * wv + 16 * (g >> 1) + (lane & 15)) * L + t0 + 8 * (g & 1);
#pragma unroll
  for (int i = 0; i < 5; ++i)
    if (i < 4 || wv < 2) *(u32x4*)(o + (size_t)i * 128 * L) = tr_frag<LN_RS>(p + i * 256);
}

DEV void mod_block(int b, const u16* c, const u16* wmod, float* modp, char* smem) {
  const int tid = threadIdx.x & 255, c16 = tid & 15, rg = tid >> 4, lane = tid & 63, wv = tid >> 6;
  const int cg = b % 27, kq = b / 27;
  u32x4 r[5]; unsigned cv[5];
#pragma unroll
  for (int i = 0; i < 5; ++i) {
    const int k = 72 * kq + (i < 4 || rg < 8 ? rg + 16 * i : 0);
    r[i] = *(const u32x4*)(wmod + (size_t)k * 3456 + 128 * cg + 8 * c16);
    cv[i] = c[k];
  }
  float acc[8] = {};
#pragma unroll
  for (int i = 0; i < 5; ++i) {
    const float x = bf2f(cv[i]);
    const float sv = i < 4 || rg < 8 ? rb(x / (1.f + __expf(-x))) : 0.f;
#pragma unroll
    for (int e = 0; e < 8; ++e) acc[e] += sv * elem(r[i], e);
  }
#pragma unroll
  for (int e = 0; e < 8; ++e) { acc[e] += __shfl_xor(acc[e], 16); acc[e] += __shfl_xor(acc[e], 32); }
  float* red = (float*)smem;
  if (lane < 16)
#pragma unroll
    for (int e = 0; e < 8; ++e) red[wv * 128 + lane * 8 + e] = acc[e];
  __syncthreads();
  if (tid < 128) modp[kq * 3456 + 128 * cg + tid] = (red[tid] + red[128 + tid]) + (red[256 + tid] + red[384 + tid]);
}

constexpr int KG_RS = 528;
DEV void kgen_block(int d, const u16* Are, const u16* Aim, const u16* Bre, const u16* Bim, const u16* Cre, const u16* Cim,
                    const u16* logdt, float* ktab, char* smem) {
  const int tid = threadIdx.x, lane = tid & 63, wv = __builtin_amdgcn_readfirstlane(tid >> 6), p = lane & 15, g = lane >> 4;
  float* scl = (float*)(smem + 57 * KG_RS);
  const int mode = tid & 127, n = d * NM + mode;
  const unsigned iare = Are[n], iaim = Aim[n], ibre = Bre[n], ibim = Bim[n], icre = Cre[n], icim = Cim[n], ildt = logdt[d];
  {
    const float dt = rb(expf(bf2f(ildt)));
    const float a2 = dt * bf2f(iare) * 1.44269504088896341f, br = dt * bf2f(iaim) * 0.15915494309189535f;
    cf cb = cmul(cf{bf2f(icre), bf2f(icim)}, cf{dt * bf2f(ibre), dt * bf2f(ibim)});
    float mx = fmaxf(fabsf(cb.re), fabsf(cb.im));
    mx = fmaxf(mx, __int_as_float(__builtin_amdgcn_update_dpp(0, __float_as_int(mx), 0xB1, 0xf, 0xf, false)));
    mx = fmaxf(mx, __int_as_float(__builtin_amdgcn_update_dpp(0, __float_as_int(mx), 0x4E, 0xf, 0xf, false)));
    mx = fmaxf(mx, __int_as_float(__builtin_amdgcn_update_dpp(0, __float_as_int(mx), 0x124, 0xf, 0xf, false)));
    mx = fmaxf(mx, __int_as_float(__builtin_amdgcn_update_dpp(0, __float_as_int(mx), 0x128, 0xf, 0xf, false)));
    { auto q = __builtin_amdgcn_permlane16_swap(__float_as_uint(mx), __float_as_uint(mx), false, false); mx = fmaxf(__uint_as_float(q[0]), __uint_as_float(q[1])); }
    int ex = (int)((__float_as_uint(mx) >> 23) & 0xff);
    ex = ex < 20 ? 20 : (ex > 230 ? 230 : ex);
    const float sc = __uint_as_float((unsigned)(127 + 12 + 127 - ex) << 23);
    cb.re *= sc; cb.im *= sc;
    if (wv < 2 && (lane & 31) == 0) scl[mode >> 5] = __uint_as_float((unsigned)(ex - 12) << 23);
    auto cexp = [&](float l) {
      const float e = __builtin_amdgcn_exp2f(a2 * l), ph = __builtin_amdgcn_fractf(br * l);
      return cf{e * __builtin_amdgcn_cosf(ph), e * __builtin_amdgcn_sinf(ph)};
    };
    char* trow = smem + 0 + mode * 4;
    auto put = [&](int row, float re, float im) {
      typedef __attribute__((ext_vector_type(2))) _Float16 f16x2;
      *(f16x2*)(trow + row * KG_RS) = __builtin_convertvector(f32x2{re, im}, f16x2);
    };
    const cf w128 = cexp(128.f);
    if (wv < 2) {
      const cf w1_ = cexp(1.f), w8 = cexp(8.f), w64 = cexp(64.f);
      cf e0 = {1.f, 0.f}, e1 = w64, x = cb;
#pragma unroll
      for (int j = 0; j < 8; ++j) {
        put(j, e0.re, -e0.im); put(8 + j, e1.re, -e1.im);
        const cf y = cmul(x, w128);
        put(16 + j, x.re, x.im); put(24 + j, y.re, y.im);
        if (j < 7) { e0 = cmul(e0, w8); e1 = cmul(e1, w8); x = cmul(x, w1_); }
      }
    } else {
      const cf w2_ = cexp(2.f), w4 = cmul(w2_, w2_);
      cf x = cmul(cb, cexp(256.f));
#pragma unroll
      for (int j = 0; j < 4; ++j) {
        const cf y = cmul(x, w128);
        put(32 + j, x.re, x.im); put(36 + j, y.re, y.im);
        if (j < 3) x = cmul(x, w2_);
      }
      x = cmul(cb, cexp(512.f));
#pragma unroll
      for (int j = 0; j < 4; ++j) {
        const cf y = cmul(x, w4);
        put(40 + 2 * j, x.re, x.im); put(41 + 2 * j, y.re, y.im);
        if (j < 3) x = cmul(x, w128);
      }
      x = cmul(cb, cexp(1024.f));
      cf z = cmul(cb, cexp(1536.f));
#pragma unroll
      for (int j = 0; j < 5; ++j) {
        if (j < 4) put(48 + j, x.re, x.im);
        put(52 + j, z.re, z.im);
        if (j < 4) { x = cmul(x, w128); z = cmul(z, w128); }
      }
    }
  }
  lds_barrier();
  if (wv < 3) {
    const int arow = 16 + 16 * wv + (wv == 2 && p > 8 ? 8 : p);
    f16x8 fa[8], fb[8];
#pragma unroll
    for (int k = 0; k < 8; ++k) {
      const char* q = smem + 0 + (16 * k + 4 * g) * 4;
      fa[k] = *(const f16x8*)(q + arow * KG_RS); fb[k] = *(const f16x8*)(q + p * KG_RS);
    }
    const f32x4 sc4 = *(const f32x4*)scl;
    f32x4 acc[4] = {};
    asm volatile("" : "+v"(acc[0]), "+v"(acc[1]), "+v"(acc[2]), "+v"(acc[3]));
    __builtin_amdgcn_sched_barrier(0);
#pragma unroll
    for (int k = 0; k < 8; ++k) acc[k >> 1] = __builtin_amdgcn_mfma_f32_16x16x32_f16(fa[k], fb[k], acc[k >> 1], 0, 0, 0);
    __builtin_amdgcn_sched_barrier(0);
    const f32x4 sum = (acc[0] * sc4[0] + acc[1] * sc4[1]) + (acc[2] * sc4[2] + acc[3] * sc4[3]);
    const int b0 = wv == 0 ? 4 * (g & 1) + 128 * (g >> 1) + 8 * p : (wv == 2 ? 512 + 64 * g + p : (g < 2 ? 256 + 64 * g + 4 * p : 256 + 64 * g + 2 * p));
    const int s1 = wv == 2 ? 16 : 1, s2 = (wv == 2 || (wv == 1 && g >= 2)) ? 32 : 2;
#pragma unroll
    for (int r = 0; r < 4; ++r) ktab[min(b0 + (r & 1) * s1 + (r >> 1) * s2, 656)] = sum[r];
  }
}

constexpr int RT0 = 192;
constexpr int K0_LDS = 57 * KG_RS + 16;
__global__ __launch_bounds__(256, 5) void k0_prep(const u16* x, const u16* Are, const u16* Aim, const u16* Bre, const u16* Bim,
    const u16* Cre, const u16* Cim, const u16* logdt, const u16* w1, const u16* w3, const u16* w2, const u16* c, const u16* wmod, char* ws) {
  extern __shared__ __attribute__((aligned(16))) char smem[];
  KT_BEGIN(1)
  const int b = blockIdx.x < 920 ? (blockIdx.x < 576 ? blockIdx.x + 344 : 919 - blockIdx.x) : blockIdx.x;
  if (b < 216) {
    mod_block(b, c, wmod, (float*)(ws + WS_MODP), smem);
  } else if (b < 344) {
    ln1_block(b - 216, x, (u16*)(ws + WS_XT), (float*)(ws + WS_ZT), smem);
    if (b == 216) *(u32x4*)(ws + WS_CNT + threadIdx.x * 16) = u32x4{0, 0, 0, 0};
  } else if (b < 344 + 576) {
    kgen_block(b - 344, Are, Aim, Bre, Bim, Cre, Cim, logdt, (float*)(ws + WS_KR) + (b - 344) * 768, smem);
  } else {
    const int u = b - 920;
    u32x4 r0[3];
    if (u < 144) retile1_load(u, w1, w3, r0); else retile2_load(u, w2, r0);
    if (u < 144) retile_stage<2>(r0, smem); else retile_stage<3>(r0, smem);
    __syncthreads();
    if (u < 144) retile1_emit(u, smem, ws + WS_WT); else retile2_emit(u, smem, ws + WS_WT);
  }
  KT_END(1, ws);
}

constexpr int CP_STRIDE = 4160;
constexpr int K1_U = 0;
constexpr int K1_TAB = 4608;
constexpr int K1_OUT = K1_TAB + 4 * CP_STRIDE;
constexpr int K1_ST = 4 * 2560 * 4, K1_LDS = K1_ST + 12288;
__global__ __launch_bounds__(256, 3) void k1_conv(const u16* Dskip, const u16* w1, const u16* w3, const u16* w2, char* ws) {
  extern __shared__ __attribute__((aligned(16))) char smem[];
  STAMP_DECL
  STAMP(0);
  const int d = blockIdx.x, tid = threadIdx.x, lane = tid & 63, wv = __builtin_amdgcn_readfirstlane(tid >> 6);
  const int p = lane & 15, g = lane >> 4;
  float* tab = (float*)(smem + K1_OUT);
  const float* ktab = (const float*)(ws + WS_KR) + d * 768 + tid;
  const float k0 = ktab[0], k1 = ktab[256], k2 = ktab[tid <= 128 ? 512 : 0];
  const u32x4 xv = *(const u32x4*)((const u16*)(ws + WS_XT) + (size_t)d * L + 8 * tid);
  const float* stat = (const float*)(ws + WS_ZT) + 8 * tid;
  const f32x4 m0 = *(const f32x4*)stat, m1 = *(const f32x4*)(stat + 4), r0 = *(const f32x4*)(stat + L), r1 = *(const f32x4*)(stat + L + 4);
  float mq = ((const float*)(ws + WS_MODP))[(lane & 7) * 3456 + min(lane >> 3, 5) * D + d];
  const float dsk = bf2f(Dskip[d]);
  __builtin_amdgcn_sched_barrier(0);
  tab[tid] = k0; tab[256 + tid] = k1;
  if (tid <= 128) tab[512 + tid] = k2;
  STAMP(1); STAMP(2);
  mq += __int_as_float(__builtin_amdgcn_update_dpp(0, __float_as_int(mq), 0xB1, 0xf, 0xf, false));
  mq += __int_as_float(__builtin_amdgcn_update_dpp(0, __float_as_int(mq), 0x4E, 0xf, 0xf, false));
  mq += __int_as_float(__builtin_amdgcn_update_dpp(0, __float_as_int(mq), 0x141, 0xf, 0xf, false));
  mq = rb(mq);
  auto bc = [&](int i) { return __int_as_float(__builtin_amdgcn_readlane(__float_as_int(mq), 8 * i)); };
  const float sa = bc(0), ca1 = rb(1.f + bc(1)), ga = bc(2);
  u32x4 hp;
#pragma unroll
  for (int e = 0; e < 4; ++e) {
    const f32x2 mm = e < 2 ? f32x2{m0[2 * e], m0[2 * e + 1]} : f32x2{m1[2 * e - 4], m1[2 * e - 3]};
    const f32x2 rr = e < 2 ? f32x2{r0[2 * e], r0[2 * e + 1]} : f32x2{r1[2 * e - 4], r1[2 * e - 3]};
    const f32x2 hh = rb2(rb2(rb2(f32x2{bflo(xv[e]), bfhi(xv[e])} - mm) * rr) * ca1) + sa;
    hp[e] = pk2(hh.x, hh.y);
  }
  *(u32x4*)(smem + K1_U + 480 + 16 * tid) = hp;
  if (tid < 30) *(u32x4*)(smem + K1_U + 16 * tid) = u32x4{0, 0, 0, 0};
  STAMP(3);
  lds_barrier();
  u32x4 rt[3];
  const bool side = (d & 255) >= 64;
  const int ru = RT0 + (d & 255) - 64 + 192 * (d >> 8);
  if (side) retile2_load(ru, w2, rt);
  unsigned pf = 0;
  if (side && wv == 3) pf = *(const unsigned*)(((d >> 8) ? w3 : w1) + (size_t)((d & 255) - 64) * F + 576 * (lane >> 3) + 64 * (lane & 7));
  __builtin_amdgcn_sched_barrier(0);
  {
    float kv[12];
#pragma unroll
    for (int i = 0; i < 12; i += 2) {
      const int l = 2047 - (8 * tid + i);
      const unsigned bits = pk2((float)l, (float)(l - 1));
      const int i0 = l < 128 ? (l < 0 ? 0 : l) : (int)(bits & 0xffff) - 17024, i1 = l - 1 < 128 ? (l < 1 ? 0 : l - 1) : (int)(bits >> 16) - 17024;
      kv[i] = tab[i0]; kv[i + 1] = tab[i1];
      if (i >= 8 && tid == 255) kv[i] = kv[i + 1] = 0.f;
    }
    unsigned w[6];
#pragma unroll
    for (int i = 0; i < 6; ++i) w[i] = pk2(kv[2 * i], kv[2 * i + 1]);
    auto al = [](unsigned hi, unsigned lo) { return __builtin_amdgcn_alignbit(hi, lo, 16); };
    char* cp = smem + K1_TAB + 16 * tid;
    *(u32x4*)(cp + 0 * CP_STRIDE) = u32x4{w[0], w[1], w[2], w[3]};
    *(u32x4*)(cp + 1 * CP_STRIDE) = u32x4{al(w[1], w[0]), al(w[2], w[1]), al(w[3], w[2]), al(w[4], w[3])};
    *(u32x4*)(cp + 2 * CP_STRIDE) = u32x4{w[1], w[2], w[3], w[4]};
    *(u32x4*)(cp + 3 * CP_STRIDE) = u32x4{al(w[2], w[1]), al(w[3], w[2]), al(w[4], w[3]), al(w[5], w[4])};
    if (tid < 8) *(u32x4*)(smem + K1_TAB + (tid >> 1) * CP_STRIDE + 4096 + 16 * (tid & 1)) = u32x4{0, 0, 0, 0};
  }
  STAMP(4);
  lds_barrier();
  const int sg = (3 - p) & 3;
  const unsigned abase = K1_TAB + sg * CP_STRIDE + 2 * (2031 + 8 * g - p - sg);
  const unsigned bbase = K1_U + 2 * (240 + 16 * p - 16 + 8 * g);
  f32x4 acc[8] = {};
#pragma unroll
  for (int i = 0; i < 8; ++i) asm volatile("" : "+v"(acc[i]));
#pragma unroll
  for (int cc = 0; cc < 2; ++cc) {
    const int c0 = 2 * wv + cc;
    bf16x8 A[8], B[8];
#pragma unroll
    for (int i = 0; i < 8; ++i) {
      const char* pa = smem + abase - 64 * c0 - 512 * i;
      u32x2 lo = *(const u32x2*)pa, hi = *(const u32x2*)(pa + 8);
      A[i] = __builtin_bit_cast(bf16x8, u32x4{lo.x, lo.y, hi.x, hi.y});
      B[i] = *(const bf16x8*)(smem + bbase - 64 * c0 + 512 * i);
    }
#pragma unroll
    for (int c1 = 0; c1 < 8; ++c1)
#pragma unroll
      for (int i = c1; i < 8; ++i) acc[i] = __builtin_amdgcn_mfma_f32_16x16x32_bf16(A[c1], B[i - c1], acc[i], 0, 0, 0);
  }
  float* red = (float*)smem;
  if (side) retile_stage<3>(rt, smem + K1_ST);
  STAMP(5);
  lds_barrier();
#pragma unroll
  for (int i = 0; i < 8; ++i) *(f32x4*)(red + wv * 2560 + (16 * i + p) * 20 + 4 * g) = acc[i];
  lds_barrier();
  STAMP(6);
  float y[8];
  {
    const int o = (tid >> 1) * 20 + (tid & 1) * 8;
#pragma unroll
    for (int hh = 0; hh < 2; ++hh) {
      f32x4 a = *(const f32x4*)(red + o + 4 * hh), b = *(const f32x4*)(red + 2560 + o + 4 * hh);
      f32x4 c = *(const f32x4*)(red + 5120 + o + 4 * hh), e = *(const f32x4*)(red + 7680 + o + 4 * hh);
      f32x4 s = (a + b) + (c + e);
#pragma unroll
      for (int k = 0; k < 4; ++k) y[4 * hh + k] = s[k];
    }
  }
  float o[8];
#pragma unroll
  for (int e = 0; e < 4; ++e) {
    const f32x2 v = f32x2{bflo(xv[e]), bfhi(xv[e])} + rb2(rb2(rb2(f32x2{y[2 * e], y[2 * e + 1]}) + rb2(f32x2{bflo(hp[e]), bfhi(hp[e])} * dsk)) * ga);
    o[2 * e] = v.x; o[2 * e + 1] = v.y;
  }
  *(u32x4*)((u16*)(ws + WS_X1T) + (size_t)d * L + 8 * tid) = u32x4{pk2(o[0], o[1]), pk2(o[2], o[3]), pk2(o[4], o[5]), pk2(o[6], o[7])};
  if (side) retile2_emit(ru, smem + K1_ST, ws + WS_WT);
  if (tid >= 24 && tid < 48 && (tid & 7) == 0) ((u16*)(ws + WS_MODF))[((tid >> 3) - 3) * D + d] = (u16)pk2(tid == 32 ? 1.f + mq : mq, 0.f);
  asm volatile("" :: "v"(pf));
  STAMP(7);
}

constexpr int K3_H = 73728, K3_ST = 2 * 73728, K3_LDS = 2 * 73728 + 8192;
#define LDIDX(i) (i)
constexpr int RING = 16;
DEV unsigned swiglu_tile(const f32x4& c) {
  auto s0 = __builtin_amdgcn_permlane32_swap(__float_as_uint(c[0]), __float_as_uint(c[2]), false, false);
  auto s1 = __builtin_amdgcn_permlane32_swap(__float_as_uint(c[1]), __float_as_uint(c[3]), false, false);
  const f32x2 gt = {__uint_as_float(s0[0]), __uint_as_float(s1[0])}, up = {__uint_as_float(s0[1]), __uint_as_float(s1[1])};
  const f32x2 ar = gt * -1.44269504088896341f;
  const f32x2 dn = f32x2{__builtin_amdgcn_exp2f(ar.x), __builtin_amdgcn_exp2f(ar.y)} + 1.f;
  const f32x2 hv = gt * f32x2{__builtin_amdgcn_rcpf(dn.x), __builtin_amdgcn_rcpf(dn.y)} * up;
  return pk2(hv.x, hv.y);
}
DEV bf16x8 tfrag(const u32x4 (&raw)[8], int i) {
  const unsigned sel = (i & 1) ? 0x07060302u : 0x05040100u;
  u32x4 o;
#pragma unroll
  for (int q = 0; q < 4; ++q) o[q] = __builtin_amdgcn_perm(raw[2 * q + 1][i >> 1], raw[2 * q][i >> 1], sel);
  return __builtin_bit_cast(bf16x8, o);
}
template <int NT>
DEV void ffn_phase2(__amdgpu_buffer_rsrc_t rs, int wv, int lane, char* smem, int s, int mt, u16* out, bf16x8 (&ring)[RING], char* ws STAMP_ARG) {
  const unsigned vo = lane * 16;
  constexpr int F0 = NT == 5 ? 36 : 0, NFR = F0 + 18 * NT;
  auto ldw = [&](int idx) { return __builtin_bit_cast(bf16x8, __builtin_amdgcn_raw_buffer_load_b128(rs, vo, idx * 1024, 0)); };
  bf16x8 b[2][4];
  f32x4 oc[NT][4];
#pragma unroll
  for (int tt = 0; tt < 4; ++tt) b[0][tt] = *(const bf16x8*)(smem + K3_H + tt * 1024 + lane * 16);
#pragma unroll
  for (int ks = 0; ks < 18; ++ks) {
    if (ks < 17) {
#pragma unroll
      for (int tt = 0; tt < 4; ++tt) b[(ks + 1) & 1][tt] = *(const bf16x8*)(smem + K3_H + (ks + 1) * 4096 + tt * 1024 + lane * 16);
    }
#pragma unroll
    for (int ti = 0; ti < NT; ++ti) {
      const int i = F0 + ks * NT + ti;
#pragma unroll
      for (int tt = 0; tt < 4; ++tt)
        oc[ti][tt] = __builtin_amdgcn_mfma_f32_16x16x32_bf16(ring[i % RING], b[ks & 1][tt], ks == 0 ? f32x4{0.f, 0.f, 0.f, 0.f} : oc[ti][tt], 0, 0, 0);
      if (i + RING < NFR) ring[i % RING] = ldw(i + RING);
      __builtin_amdgcn_sched_barrier(0);
    }
  }
  STAMP(8);
  const int dt0 = NT == 5 ? 5 * wv : 20 + 4 * (wv - 4);
  char* pbase = ws + WS_P + (size_t)mt * 8 * 73728;
  __amdgpu_buffer_rsrc_t rp = __builtin_amdgcn_make_buffer_rsrc(pbase, 0, 8 * 73728, 0x00020000);
#pragma unroll
  for (int ti = 0; ti < NT; ++ti)
#pragma unroll
    for (int tp = 0; tp < 2; ++tp) {
      const f32x4 u = oc[ti][2 * tp], v = oc[ti][2 * tp + 1];
      __builtin_amdgcn_raw_buffer_store_b128(u32x4{pk2(u[0], u[1]), pk2(u[2], u[3]), pk2(v[0], v[1]), pk2(v[2], v[3])}, rp,
                                             vo + (s * 72 + dt0 * 2) * 1024 + (ti * 2 + tp) * 1024, 0, 16  );
    }
  const u16* x1T = (const u16*)(ws + WS_X1T);
  const u16* mf = (const u16*)(ws + WS_MODF);
  constexpr int NU = NT == 5 ? 1 : 2;
  const int nu = wv == 7 ? 2 : 1;
  unsigned xr[NU][8], gb[NU][2];
#pragma unroll
  for (int k = 0; k < NU; ++k)
    if (k < nu) {
      const int un = 9 * s + wv + k, d0 = 16 * (un >> 1) + 4 * (lane >> 4), t0 = 64 * mt + 32 * (un & 1) + (lane & 15);
#pragma unroll
      for (int e = 0; e < 4; ++e) { xr[k][e] = x1T[(size_t)(d0 + e) * L + t0]; xr[k][4 + e] = x1T[(size_t)(d0 + e) * L + t0 + 16]; }
      const u32x2 gg = *(const u32x2*)(mf + 2 * D + d0);
      gb[k][0] = gg.x; gb[k][1] = gg.y;
    }
  asm volatile("s_waitcnt vmcnt(0)" ::: "memory");
  STAMP(9);
  __builtin_amdgcn_s_barrier();
  {
    const char* cnt = ws + WS_CNT + mt * 128;
    unsigned zero = 0, one = 1, tmp, sv, left;
    unsigned long long ex;
    asm volatile(
        "s_cmp_lg_u32 %[wv], 0\n\t"
        "s_cbranch_scc1 2f\n\t"
        "s_mov_b64 %[ex], exec\n\t"
        "s_mov_b64 exec, 1\n\t"
        "s_nop 1\n\t"
        "global_atomic_add %[zero], %[one], %[cnt]\n\t"
        "s_mov_b32 %[left], 0x100000\n\t"
        "1:\n\t"
        "global_load_dword %[tmp], %[zero], %[cnt] sc1\n\t"
        "s_waitcnt vmcnt(0)\n\t"
        "v_readfirstlane_b32 %[sv], %[tmp]\n\t"
        "s_cmp_ge_u32 %[sv], 8\n\t"
        "s_cbranch_scc1 3f\n\t"
        "s_sleep 2\n\t"
        "s_sub_u32 %[left], %[left], 1\n\t"
        "s_cmp_lg_u32 %[left], 0\n\t"
        "s_cbranch_scc1 1b\n\t"
        "3:\n\t"
        "s_mov_b64 exec, %[ex]\n\t"
        "2:\n\t"
        : [tmp] "=&v"(tmp), [sv] "=&s"(sv), [left] "=&s"(left), [ex] "=&s"(ex)
        : [wv] "s"(wv), [zero] "v"(zero), [one] "v"(one), [cnt] "s"(cnt)
        : "memory", "scc");
  }
  asm volatile("" ::: "memory");
  __builtin_amdgcn_s_barrier();
  asm volatile("" ::: "memory");
  STAMP(10);
  u32x4 pv[NU][8];
#pragma unroll
  for (int k = 0; k < NU; ++k)
    if (k < nu)
#pragma unroll
      for (int q = 0; q < 8; ++q) pv[k][q] = __builtin_amdgcn_raw_buffer_load_b128(rp, vo, q * 73728 + (9 * s + wv + k) * 1024, 16  );
#pragma unroll
  for (int k = 0; k < NU; ++k)
    if (k < nu) {
      const int un = 9 * s + wv + k, d0 = 16 * (un >> 1) + 4 * (lane >> 4), t0 = 64 * mt + 32 * (un & 1) + (lane & 15);
      float o[8];
#pragma unroll
      for (int e = 0; e < 8; ++e) {
        const float a = ((elem(pv[k][0], e) + elem(pv[k][1], e)) + (elem(pv[k][2], e) + elem(pv[k][3], e))) +
                        ((elem(pv[k][4], e) + elem(pv[k][5], e)) + (elem(pv[k][6], e) + elem(pv[k][7], e)));
        const unsigned gw = gb[k][(e & 3) >> 1];
        o[e] = bf2f(xr[k][e]) + rb(((e & 1) ? bfhi(gw) : bflo(gw)) * rb(a));
      }
      *(u32x2*)(out + (size_t)t0 * D + d0) = u32x2{pk2(o[0], o[1]), pk2(o[2], o[3])};
      *(u32x2*)(out + (size_t)(t0 + 16) * D + d0) = u32x2{pk2(o[4], o[5]), pk2(o[6], o[7])};
    }
  STAMP(11);
  STAMP_OUT(ws, 12);
}

DEV void ffn_phase1(const char* wp, const char* whead, const char* pfp, __amdgpu_buffer_rsrc_t rs, int wv, int lane, char* smem, u32x4 (&raw)[2][8], int s, int mt, u16* out, char* ws STAMP_ARG) {
  bf16x8 ring[RING];
  const unsigned vo = lane * 16;
  auto ldw = [&](int idx) { return __builtin_bit_cast(bf16x8, __builtin_amdgcn_raw_buffer_load_b128(rs, vo, idx * 1024, 0)); };
  unsigned boff[4];
#pragma unroll
  for (int tt = 0; tt < 4; ++tt) boff[tt] = tt * 1024 + ((lane ^ ((2 * tt + ((lane >> 3) & 1)) & 7)) * 16);
  f32x4 acc[8][4] = {};
  bf16x8 b[4];
#pragma unroll
  for (int tt = 0; tt < 4; ++tt) b[tt] = *(const bf16x8*)(smem + boff[tt]);
  bf16x8 a = tfrag(raw[0], 0);
  auto ldrow = [&](int j) {
    const char* p;
    asm("v_lshl_add_u64 %0, %1, 0, %2" : "=v"(p) : "v"(wp), "s"((unsigned long long)j * (F * 2)));
    return *(const __attribute__((address_space(1))) u32x4*)p;
  };
#define SGB(m, n) __builtin_amdgcn_sched_group_barrier(m, n, 0)
  auto step = [&](int par, int ksn, unsigned inc) {
    bf16x8 hi[4];
#pragma unroll
    for (int i = 0; i < 4; ++i) {
      bf16x8 an;
      if (i < 3) an = tfrag(raw[par], i + 1);
      hi[i] = tfrag(raw[par], 4 + i);
#pragma unroll
      for (int tt = 0; tt < 4; ++tt) acc[i][tt] = __builtin_amdgcn_mfma_f32_16x16x32_bf16(a, b[tt], acc[i][tt], 0, 0, 0);
      if (i < 3) a = an;
      SGB(0x8, 1); SGB(0x2, 2); SGB(0x8, 1); SGB(0x2, 2); SGB(0x8, 1); SGB(0x2, 2); SGB(0x8, 1); SGB(0x2, 2);
      __builtin_amdgcn_sched_barrier(0);
    }
#pragma unroll
    for (int i = 4; i < 6; ++i) {
#pragma unroll
      for (int j = 0; j < 3; ++j) raw[par][3 * (i - 4) + j] = ldrow(3 * (i - 4) + j);
#pragma unroll
      for (int tt = 0; tt < 4; ++tt) acc[i][tt] = __builtin_amdgcn_mfma_f32_16x16x32_bf16(hi[i - 4], b[tt], acc[i][tt], 0, 0, 0);
      SGB(0x8, 1); SGB(0x2, 1); SGB(0x20, 1); SGB(0x8, 1); SGB(0x2, 1); SGB(0x20, 1); SGB(0x8, 1); SGB(0x2, 1); SGB(0x20, 1); SGB(0x8, 1);
      __builtin_amdgcn_sched_barrier(0);
    }
    const char* bn = smem + ksn * 4096;
#pragma unroll
    for (int tt = 0; tt < 4; ++tt) {
      if (tt < 2) raw[par][6 + tt] = ldrow(6 + tt);
      if (tt == 2) { a = tfrag(raw[par ^ 1], 0); wp += inc; }
      acc[6][tt] = __builtin_amdgcn_mfma_f32_16x16x32_bf16(hi[2], b[tt], acc[6][tt], 0, 0, 0);
      acc[7][tt] = __builtin_amdgcn_mfma_f32_16x16x32_bf16(hi[3], b[tt], acc[7][tt], 0, 0, 0);
      b[tt] = *(const bf16x8*)(bn + boff[tt]);
      SGB(0x8, 1); SGB(0x2, 3); SGB(0x20, 1); SGB(0x8, 1); SGB(0x2, 3); SGB(0x100, 1);
      __builtin_amdgcn_sched_barrier(0);
    }
  };
  unsigned pf = 0;
#pragma unroll 1
  for (int it = 0; it < 9; ++it) {
    asm volatile("" :: "v"(pf));
    if (it < 6 && wv == it) pf = *(const unsigned*)(pfp + (size_t)it * 64 * (F * 2));
    const bool last = it == 8;
    if (last) wp = whead;
    const unsigned inc = (last ? 8 : 32) * (F * 2);
    step(0, 2 * it + 1, inc);
    step(1, last ? 0 : 2 * it + 2, inc);
  }
#pragma unroll
  for (int i = 0; i < 16; ++i) ring[i] = __builtin_bit_cast(bf16x8, raw[i >> 3][i & 7]);
  STAMP(5);
  char* hw0 = smem + K3_H + (2 * wv) * 4096 + lane * 16;
  auto put4 = [&](int h, int tt) {
    *(u32x4*)(hw0 + h * 4096 + tt * 1024) = u32x4{swiglu_tile(acc[4 * h][tt]), swiglu_tile(acc[4 * h + 1][tt]), swiglu_tile(acc[4 * h + 2][tt]), swiglu_tile(acc[4 * h + 3][tt])};
  };
  if (wv < 4) {
    put4(0, 0); put4(0, 1);
    __builtin_amdgcn_sched_barrier(0);
    f32x4 am[2][4] = {};
#pragma unroll
    for (int ks = 0; ks < 18; ++ks) {
#pragma unroll
      for (int tt = 0; tt < 4; ++tt) {
        am[0][tt] = __builtin_amdgcn_mfma_f32_16x16x32_bf16(ring[(2 * ks) % RING], b[tt], am[0][tt], 0, 0, 0);
        am[1][tt] = __builtin_amdgcn_mfma_f32_16x16x32_bf16(ring[(2 * ks + 1) % RING], b[tt], am[1][tt], 0, 0, 0);
        if (ks < 17) b[tt] = *(const bf16x8*)(smem + (ks + 1) * 4096 + boff[tt]);
      }
      ring[(2 * ks) % RING] = ldw(2 * ks + RING);
      ring[(2 * ks + 1) % RING] = ldw(2 * ks + 1 + RING);
      if (ks < 18 && ks % 3 == 0) put4((ks / 3 + 2) >> 2, (ks / 3 + 2) & 3);
      __builtin_amdgcn_sched_barrier(0);
    }
    char* hw8 = smem + K3_H + (16 + (wv >> 1)) * 4096 + lane * 16 + (wv & 1) * 8;
#pragma unroll
    for (int tt = 0; tt < 4; ++tt) *(u32x2*)(hw8 + tt * 1024) = u32x2{swiglu_tile(am[0][tt]), swiglu_tile(am[1][tt])};
    STAMP(6);
    __syncthreads();
    STAMP(7);
    ffn_phase2<5>(rs, wv, lane, smem, s, mt, out, ring, ws STAMP_PASS);
    __builtin_amdgcn_endpgm();
  }
#pragma unroll
  for (int q = 0; q < 8; ++q) put4(q >> 2, q & 3);
  STAMP(6);
  __syncthreads();
  STAMP(7);
  ffn_phase2<4>(rs, wv, lane, smem, s, mt, out, ring, ws STAMP_PASS);
  __builtin_amdgcn_endpgm();
}
__global__ __launch_bounds__(512, 2) void k3_ffn(const u16* w1, const u16* w3, char* ws, u16* out) {
  extern __shared__ __attribute__((aligned(16))) char smem[];
  const int tid = threadIdx.x, lane = tid & 63;
  const int wv = __builtin_amdgcn_readfirstlane(tid >> 6);
  STAMP_DECL
  STAMP(0);
  const int xc = blockIdx.x & 7, bi = blockIdx.x >> 3;
  const int s = (xc % (8 / SPX)) * SPX + bi % SPX, mt = (xc / (8 / SPX)) * (32 / SPX) + bi / SPX;
  const u16* x1T = (const u16*)(ws + WS_X1T) + 64 * mt;
  const u16* mf = (const u16*)(ws + WS_MODF);
  const int tc = tid & 7, cg = tid >> 3;
  u32x4 r[9];
#pragma unroll
  for (int e = 0; e < 8; ++e) r[e] = *(const u32x4*)(x1T + (size_t)(8 * cg + e) * L + 8 * tc);
  r[8] = *(const u32x4*)(x1T + (size_t)(512 + cg) * L + 8 * tc);
  const u32x4 sbv = *(const u32x4*)(mf + 8 * cg), cbv = *(const u32x4*)(mf + D + 8 * cg);
  const float sb8 = bf2f(mf[512 + cg]), cb8 = bf2f(mf[D + 512 + cg]);
  __builtin_amdgcn_sched_barrier(0);
  asm volatile("s_waitcnt vmcnt(0)" ::: "memory");
  STAMP(1);
  __builtin_amdgcn_sched_barrier(0);
  f32x2 xv[9][4];
#pragma unroll
  for (int e = 0; e < 9; ++e)
#pragma unroll
    for (int t = 0; t < 4; ++t) xv[e][t] = f32x2{bflo(r[e][t]), bfhi(r[e][t])};
  float s1, s2;
  {
    f32x2 p1[4], p2[4];
#pragma unroll
    for (int t = 0; t < 4; ++t) {
      p1[t] = xv[0][t]; p2[t] = xv[0][t] * xv[0][t];
#pragma unroll
      for (int e = 1; e < 9; ++e) { p1[t] += xv[e][t]; p2[t] += xv[e][t] * xv[e][t]; }
    }
    auto sw32 = [](float a, float b) { auto q = __builtin_amdgcn_permlane32_swap(__float_as_uint(a), __float_as_uint(b), false, false); return __uint_as_float(q[0]) + __uint_as_float(q[1]); };
    auto sw16 = [](float a, float b) { auto q = __builtin_amdgcn_permlane16_swap(__float_as_uint(a), __float_as_uint(b), false, false); return __uint_as_float(q[0]) + __uint_as_float(q[1]); };
    float u1[4], u2[4];
#pragma unroll
    for (int t = 0; t < 4; ++t) { u1[t] = sw32(p1[t >> 1][t & 1], p1[2 + (t >> 1)][t & 1]); u2[t] = sw32(p2[t >> 1][t & 1], p2[2 + (t >> 1)][t & 1]); }
    const float v10 = sw16(u1[0], u1[2]), v11 = sw16(u1[1], u1[3]), v20 = sw16(u2[0], u2[2]), v21 = sw16(u2[1], u2[3]);
    const bool b3 = lane & 8;
    auto ror8 = [](float v) { return __int_as_float(__builtin_amdgcn_update_dpp(0, __float_as_int(v), 0x128, 0xf, 0xf, false)); };
    s1 = (b3 ? v11 : v10) + ror8(b3 ? v10 : v11);
    s2 = (b3 ? v21 : v20) + ror8(b3 ? v20 : v21);
  }
  float* st = (float*)(smem + K3_ST);
  *(f32x2*)(st + (wv * 64 + 8 * tc + (lane >> 3)) * 2) = f32x2{s1, s2};
  __builtin_amdgcn_sched_barrier(0);
  char* wbase = ws + WS_WT + ((size_t)s * FR_S + wave_frag_off(wv)) * 1024;
  __amdgpu_buffer_rsrc_t rs = __builtin_amdgcn_make_buffer_rsrc(wbase, 0, 126 * 1024, 0x00020000);
  const char* wp = (const char*)((lane & 8) ? w3 : w1) + ((size_t)(8 * (lane >> 4)) * F + 576 * s + 64 * wv + 8 * (lane & 7)) * 2;
  u32x4 raw[2][8];
#pragma unroll
  for (int i = 0; i < 2; ++i) {
#pragma unroll
    for (int j = 0; j < 8; ++j) raw[i][j] = *(const u32x4*)(wp + (size_t)j * (F * 2));
    wp += (size_t)32 * (F * 2);
  }
  __builtin_amdgcn_sched_barrier(0);
  STAMP(2);
  asm volatile("s_waitcnt lgkmcnt(0)" ::: "memory");
  __builtin_amdgcn_s_barrier();
  STAMP(3);
  float* sw = (float*)(smem + K3_ST + 4096) + wv * 128;
  {
    float a = 0.f, b = 0.f;
#pragma unroll
    for (int w = 0; w < 8; ++w) { f32x2 v = *(const f32x2*)(st + (w * 64 + lane) * 2); a += v.x; b += v.y; }
    const float mean = a * (1.f / D);
    const float var = fmaxf(b * (1.f / D) - mean * mean, 0.f);
    const float rd = 1.f / rb(sqrtf(rb(rb(var) + 1e-5f)));
    sw[lane] = rd; sw[64 + lane] = RLN2 ? -rb(mean) : -rb(mean) * rd;
  }
  const f32x4 rd0 = *(const f32x4*)(sw + 8 * tc), rd1 = *(const f32x4*)(sw + 8 * tc + 4);
  const f32x4 nm0 = *(const f32x4*)(sw + 64 + 8 * tc), nm1 = *(const f32x4*)(sw + 64 + 8 * tc + 4);
  const f32x2 rdp[4] = {{rd0[0], rd0[1]}, {rd0[2], rd0[3]}, {rd1[0], rd1[1]}, {rd1[2], rd1[3]}};
  const f32x2 nmp[4] = {{nm0[0], nm0[1]}, {nm0[2], nm0[3]}, {nm1[0], nm1[1]}, {nm1[2], nm1[3]}};
  char* base = smem + (cg >> 2) * 4096 + (tc >> 1) * 1024 + (cg & 3) * 256 + (tc & 1) * 128;
  char* base8 = smem + (16 + (wv >> 2)) * 4096 + (tc >> 1) * 1024 + (wv & 3) * 256 + (tc & 1) * 128 + (cg & 7) * 2;
#pragma unroll
  for (int t = 0; t < 4; ++t) {
    f32x2 hv[9];
#pragma unroll
    for (int e = 0; e < 9; ++e) {
      const float cb = e < 8 ? elem(cbv, e) : cb8, sb = e < 8 ? elem(sbv, e) : sb8;
      hv[e] = __builtin_elementwise_fma(__builtin_elementwise_fma(xv[e][t], rdp[t], nmp[t]), f32x2{cb, cb}, f32x2{sb, sb});
    }
#pragma unroll
    for (int p = 0; p < 2; ++p) {
      *(u32x4*)(base + (((2 * t + p) ^ tc) * 16)) = u32x4{pk2(hv[0][p], hv[1][p]), pk2(hv[2][p], hv[3][p]), pk2(hv[4][p], hv[5][p]), pk2(hv[6][p], hv[7][p])};
      *(u16*)(base8 + (((2 * t + p) ^ tc) * 16)) = (u16)pk2(hv[8][p], 0.f);
    }
  }
  asm volatile("s_waitcnt lgkmcnt(0)" ::: "memory");
  __builtin_amdgcn_s_barrier();
  STAMP(4);
  const char* pfp = (const char*)((lane & 8) ? w3 : w1) + ((size_t)(192 + 4 * (bi / SPX) + (lane >> 4)) * F + 576 * s + 64 * (lane & 7)) * 2;
  ffn_phase1(wp, ws + WS_WT + ((size_t)s * FR_S + wv) * 1024 + lane * 16, pfp, rs, wv, lane, smem, raw, s, mt, out, ws STAMP_PASS);
}

extern "C" void kernel_launch(void* const* d_in, const int* in_sizes, int n_in, void* d_out, int out_size, void* d_ws,
                              size_t ws_size, hipStream_t stream) {
  const u16* x = (const u16*)d_in[0];
  const u16 *Are = (const u16*)d_in[1], *Aim = (const u16*)d_in[2], *Bre = (const u16*)d_in[3], *Bim = (const u16*)d_in[4];
  const u16 *Cre = (const u16*)d_in[5], *Cim = (const u16*)d_in[6], *logdt = (const u16*)d_in[7], *Dskip = (const u16*)d_in[8];
  const u16 *w1 = (const u16*)d_in[10], *w3 = (const u16*)d_in[11], *w2 = (const u16*)d_in[12];
  const u16 *c = (const u16*)d_in[13], *wmod = (const u16*)d_in[14];
  char* ws = (char*)d_ws;
  (void)hipFuncSetAttribute((const void*)k3_ffn, hipFuncAttributeMaxDynamicSharedMemorySize, K3_LDS);
  k0_prep<<<920 + RT0, 256, K0_LDS, stream>>>(x, Are, Aim, Bre, Bim, Cre, Cim, logdt, w1, w3, w2, c, wmod, ws);
  k1_conv<<<D, 256, K1_LDS, stream>>>(Dskip, w1, w3, w2, ws);
  k3_ffn<<<256, 512, K3_LDS, stream>>>(w1, w3, ws, (u16*)d_out);
}
```

```cpp
#include <hip/hip_runtime.h>
#include <hip/hip_bf16.h>
#include <stdint.h>

typedef __attribute__((ext_vector_type(8))) __bf16 bf16x8;
typedef __attribute__((ext_vector_type(2))) __bf16 bf16x2;
typedef __attribute__((ext_vector_type(8))) _Float16 f16x8;
typedef __attribute__((ext_vector_type(4))) float f32x4;
typedef __attribute__((ext_vector_type(2))) float f32x2;
typedef __attribute__((ext_vector_type(4))) unsigned u32x4;
typedef __attribute__((ext_vector_type(2))) unsigned u32x2;
typedef __attribute__((ext_vector_type(4))) short s16x4;
typedef unsigned short u16;
#define LDSP __attribute__((address_space(3)))
#define DEV __device__ __forceinline__

#define STAMP(i)
#define STAMP_DECL
#define STAMP_ARG
#define STAMP_PASS
#define STAMP_OUT(ws, n)
#define KT_BEGIN(k)
#define KT_END(k, ws)
#define K2PRIO 1
#define PREF1 0
#define RLN2 0
#define RX1 1
#define RSW 0
#define PFW 3
#define NTL 1
#define NTP 1
#define NTS 0
#define LBAR 0
#define PREF 0
#define PREF2 0
#define SPX 4
constexpr int L = 2048, D = 576, NM = 128, F = 4608;
constexpr int KR_STRIDE = 2064;
constexpr int MODK = 8;
constexpr int FR_S = 144 + 664;

constexpr size_t WS_MODP = 0;
constexpr size_t WS_MODF = 114688;
constexpr size_t WS_CNT  = 118784;
constexpr size_t WS_XT   = 131072;
constexpr size_t WS_ZT   = WS_XT + (size_t)D * L * 2;
constexpr size_t WS_X1T  = WS_ZT + (size_t)D * L * 2;
constexpr size_t WS_KR   = WS_X1T + (size_t)D * L * 2;
constexpr size_t WS_WT   = (WS_KR + (size_t)D * KR_STRIDE * 2 + 4095) / 4096 * 4096;
constexpr size_t WS_P    = WS_WT + (size_t)8 * FR_S * 1024;

DEV float bf2f(unsigned v) { return __uint_as_float(v << 16); }
DEV float bflo(unsigned v) { return __uint_as_float(v << 16); }
DEV float bfhi(unsigned v) { return __uint_as_float(v & 0xffff0000u); }
DEV float rb(float x) { return (float)(__bf16)x; }
DEV f32x2 rb2(f32x2 x) { const f32x2 p = x * 65537.f; return __builtin_elementwise_fma(x, f32x2{-65536.f, -65536.f}, p); }
DEV unsigned pk2(float a, float b) { f32x2 v = {a, b}; bf16x2 r = __builtin_convertvector(v, bf16x2); return __builtin_bit_cast(unsigned, r); }
#define LDNT(p) __builtin_nontemporal_load(p)
#define LDP(p) __builtin_nontemporal_load(p)
#define STNT(v, p) (*(p) = (v))
DEV float elem(const u32x4& v, int i) { unsigned w = v[i >> 1]; return (i & 1) ? bfhi(w) : bflo(w); }
DEV int wave_frag_off(int w) { return 144 - 16 + (w < 4 ? 110 * w : 440 + 56 * (w - 4)); }
DEV int frag_pos(int s, int w, int i) { return s * FR_S + (i < 16 ? i * 9 + w : wave_frag_off(w) + i); }

DEV void lds_barrier() { asm volatile("s_waitcnt lgkmcnt(0)" ::: "memory"); __builtin_amdgcn_s_barrier(); asm volatile("" ::: "memory"); }
DEV void retile1_load(int u, const u16* w1, const u16* w3, u32x4 (&r)[3]) {
  const int tid = threadIdx.x & 255, s = u & 7, ks = u >> 3;
  const size_t o = (size_t)(32 * ks + (tid >> 3)) * F + 576 * s + 512 + (tid & 7) * 8;
  r[0] = LDNT((const u32x4*)(w1 + o));
  r[1] = LDNT((const u32x4*)(w3 + o));
}
DEV void retile2_load(int u, const u16* w2, u32x4 (&r)[3]) {
  const int tid = threadIdx.x & 255, s = u & 7, v = (u >> 3) - 18, ks = (v * 43) >> 7, th = v - 3 * ks;
  const int kk = tid >> 3, gg = kk >> 3, jj = kk & 7, cp = 4 * (gg & 1) + 2 * (gg >> 1) + (jj & 1);
  const bool nrm = ks < 16;
  const int f = (nrm ? 64 * (ks >> 1) + 4 * (ks & 1) : 32 * ks) + (nrm ? 8 * cp + (jj >> 1) : cp + 8 * (jj >> 1));
  const u16* p = w2 + (size_t)(576 * s + f) * D + 192 * th + (tid & 7) * 8;
#pragma unroll
  for (int i = 0; i < 3; ++i) r[i] = LDNT((const u32x4*)(p + 64 * i));
}
template <int N> DEV void retile_stage(const u32x4 (&r)[3], char* st) {
  const int tid = threadIdx.x & 255;
  if (N == 2) { *(u32x4*)(st + tid * 16) = r[0]; *(u32x4*)(st + 4096 + tid * 16) = r[1]; }
  else {
#pragma unroll
    for (int i = 0; i < 3; ++i) *(u32x4*)(st + (tid >> 3) * 384 + (tid & 7) * 16 + 128 * i) = r[i];
  }
}
template <int RS> DEV u32x4 tr_frag(const char* p) {
  s16x4 lo = __builtin_amdgcn_ds_read_tr16_b64_v4i16((LDSP s16x4*)p);
  s16x4 hi = __builtin_amdgcn_ds_read_tr16_b64_v4i16((LDSP s16x4*)(p + 4 * RS));
  u32x2 l2 = __builtin_bit_cast(u32x2, lo), h2 = __builtin_bit_cast(u32x2, hi);
  return u32x4{l2.x, l2.y, h2.x, h2.y};
}
DEV void retile1_emit(int u, const char* st, char* wt) {
  const int lane = threadIdx.x & 63, wv = __builtin_amdgcn_readfirstlane((threadIdx.x >> 6) & 3);
  const int s = u & 7, ks = u >> 3;
  const int g = lane >> 4, qq = (lane & 15) >> 2, pp = lane & 3;
  const char* p = st + (pp >> 1) * 4096 + (8 * g + qq) * 128 + (pp & 1) * 8 + wv * 32;
#pragma unroll
  for (int i = 0; i < 2; ++i)
    STNT(tr_frag<128>(p + i * 16), (u32x4*)(wt + (size_t)frag_pos(s, wv, 2 * ks + i) * 1024 + lane * 16));
}
DEV void retile2_emit(int u, const char* st, char* wt) {
  const int lane = threadIdx.x & 63, wv = __builtin_amdgcn_readfirstlane((threadIdx.x >> 6) & 3);
  const int s = u & 7, v = (u >> 3) - 18, ks = (v * 43) >> 7, th = v - 3 * ks;
  const int g = lane >> 4, qq = (lane & 15) >> 2, pp = lane & 3;
  const char* p = st + (8 * g + qq) * 384 + pp * 8 + wv * 96;
#pragma unroll
  for (int i = 0; i < 3; ++i) {
    const int dt = 12 * th + wv * 3 + i;
    const int wa = (dt * 13) >> 6, ta = dt - 5 * wa, pa = 128 + 110 * wa + 36 + (ta < 3 ? ks * 3 + ta : 54 + ks * 2 + ta - 3);
    const int wb = (dt - 20) >> 2, tb = (dt - 20) & 3, ib = (tb >> 1) * 36 + ks * 2 + (tb & 1);
    const int pb = ib < 16 ? ib * 9 + 4 + wb : 128 + 440 + 56 * wb + ib;
    const int pos = __builtin_amdgcn_readfirstlane(s * FR_S + (dt < 20 ? pa : pb));
    STNT(tr_frag<384>(p + i * 32), (u32x4*)(wt + (size_t)pos * 1024 + lane * 16));
  }
}

#define RT_K1 144
struct cf { float re, im; };
DEV cf cmul(cf a, cf b) {
  cf r;
  asm("v_mul_f32 %0, %2, %4\n\tv_mul_f32 %1, %2, %5\n\tv_fma_f32 %0, -%3, %5, %0\n\tv_fma_f32 %1, %3, %4, %1"
      : "=&v"(r.re), "=&v"(r.im) : "v"(a.re), "v"(a.im), "v"(b.re), "v"(b.im));
  return r;
}
constexpr int LN_RS = 1152 + 16;
DEV void ln1_block(int u, const u16* x, u16* xT, float* stat, char* smem) {
  const int tid = threadIdx.x & 255, tok = tid >> 4, c16 = tid & 15, lane = tid & 63, wv = __builtin_amdgcn_readfirstlane(tid >> 6);
  const int t0 = 16 * ((u & 7) * 16 + (u >> 3));
  const u16* xr = x + (size_t)(t0 + tok) * D + c16 * 8;
  u32x4 r[5];
#pragma unroll
  for (int i = 0; i < 4; ++i) r[i] = LDNT((const u32x4*)(xr + 128 * i));
  r[4] = c16 < 8 ? LDNT((const u32x4*)(xr + 512)) : u32x4{0, 0, 0, 0};
#pragma unroll
  for (int i = 0; i < 4; ++i) *(u32x4*)(smem + tok * LN_RS + (c16 + 16 * i) * 16) = r[i];
  if (c16 < 8) *(u32x4*)(smem + tok * LN_RS + (c16 + 64) * 16) = r[4];
  f32x2 sp = {0.f, 0.f};
#pragma unroll
  for (int i = 0; i < 5; ++i)
#pragma unroll
    for (int e = 0; e < 4; ++e) sp += f32x2{bflo(r[i][e]), bfhi(r[i][e])};
  auto sum16 = [](float v) {
    v += __int_as_float(__builtin_amdgcn_update_dpp(0, __float_as_int(v), 0xB1, 0xf, 0xf, false));
    v += __int_as_float(__builtin_amdgcn_update_dpp(0, __float_as_int(v), 0x4E, 0xf, 0xf, false));
    v += __int_as_float(__builtin_amdgcn_update_dpp(0, __float_as_int(v), 0x124, 0xf, 0xf, false));
    v += __int_as_float(__builtin_amdgcn_update_dpp(0, __float_as_int(v), 0x128, 0xf, 0xf, false));
    return v;
  };
  const float mean = sum16(sp.x + sp.y) * (1.f / D);
  f32x2 qp = {0.f, 0.f};
#pragma unroll
  for (int i = 0; i < 5; ++i)
#pragma unroll
    for (int e = 0; e < 4; ++e) {
      f32x2 dl = f32x2{bflo(r[i][e]), bfhi(r[i][e])} - mean;
      if (i == 4 && c16 >= 8) dl = f32x2{0.f, 0.f};
      qp += dl * dl;
    }
  const float q = sum16(qp.x + qp.y);
  if (c16 == 0) { stat[t0 + tok] = rb(mean); stat[L + t0 + tok] = 1.f / rb(sqrtf(rb(rb(q * (1.f / D)) + 1e-5f))); }
  __syncthreads();
  const int g = lane >> 4, qq = (lane & 15) >> 2, pp = lane & 3;
  const char* p = smem + (8 * (g & 1) + qq) * LN_RS + pp * 8 + (g >> 1) * 32 + wv * 64;
  u16* o = xT + (size_t)(32 * wv + 16 * (g >> 1) + (lane & 15)) * L + t0 + 8 * (g & 1);
#pragma unroll
  for (int i = 0; i < 5; ++i)
    if (i < 4 || wv < 2) *(u32x4*)(o + (size_t)i * 128 * L) = tr_frag<LN_RS>(p + i * 256);
}

DEV void mod_block(int b, const u16* c, const u16* wmod, float* modp, char* smem) {
  const int tid = threadIdx.x & 255, c16 = tid & 15, rg = tid >> 4, lane = tid & 63, wv = tid >> 6;
  const int cg = b % 27, kq = b / 27;
  u32x4 r[5]; unsigned cv[5];
#pragma unroll
  for (int i = 0; i < 5; ++i) {
    const int k = 72 * kq + (i < 4 || rg < 8 ? rg + 16 * i : 0);
    r[i] = LDNT((const u32x4*)(wmod + (size_t)k * 3456 + 128 * cg + 8 * c16));
    cv[i] = c[k];
  }
  float acc[8] = {};
#pragma unroll
  for (int i = 0; i < 5; ++i) {
    const float x = bf2f(cv[i]);
    const float sv = i < 4 || rg < 8 ? rb(x / (1.f + __expf(-x))) : 0.f;
#pragma unroll
    for (int e = 0; e < 8; ++e) acc[e] += sv * elem(r[i], e);
  }
#pragma unroll
  for (int e = 0; e < 8; ++e) { acc[e] += __shfl_xor(acc[e], 16); acc[e] += __shfl_xor(acc[e], 32); }
  float* red = (float*)smem;
  if (lane < 16)
#pragma unroll
    for (int e = 0; e < 8; ++e) red[wv * 128 + lane * 8 + e] = acc[e];
  __syncthreads();
  if (tid < 128) modp[kq * 3456 + 128 * cg + tid] = (red[tid] + red[128 + tid]) + (red[256 + tid] + red[384 + tid]);
}

constexpr int KG_RS = 528;
DEV void kgen_block(int d, const u16* Are, const u16* Aim, const u16* Bre, const u16* Bim, const u16* Cre, const u16* Cim,
                    const u16* logdt, float* ktab, char* smem) {
  const int tid = threadIdx.x, lane = tid & 63, wv = __builtin_amdgcn_readfirstlane(tid >> 6), p = lane & 15, g = lane >> 4;
  float* scl = (float*)(smem + 57 * KG_RS);
  const int mode = tid & 127, n = d * NM + mode;
  const unsigned iare = LDP(Are + n), iaim = LDP(Aim + n), ibre = LDP(Bre + n), ibim = LDP(Bim + n), icre = LDP(Cre + n), icim = LDP(Cim + n), ildt = logdt[d];
  {
    const float dt = rb(expf(bf2f(ildt)));
    const float a2 = dt * bf2f(iare) * 1.44269504088896341f, br = dt * bf2f(iaim) * 0.15915494309189535f;
    cf cb = cmul(cf{bf2f(icre), bf2f(icim)}, cf{dt * bf2f(ibre), dt * bf2f(ibim)});
    float mx = fmaxf(fabsf(cb.re), fabsf(cb.im));
    mx = fmaxf(mx, __int_as_float(__builtin_amdgcn_update_dpp(0, __float_as_int(mx), 0xB1, 0xf, 0xf, false)));
    mx = fmaxf(mx, __int_as_float(__builtin_amdgcn_update_dpp(0, __float_as_int(mx), 0x4E, 0xf, 0xf, false)));
    mx = fmaxf(mx, __int_as_float(__builtin_amdgcn_update_dpp(0, __float_as_int(mx), 0x124, 0xf, 0xf, false)));
    mx = fmaxf(mx, __int_as_float(__builtin_amdgcn_update_dpp(0, __float_as_int(mx), 0x128, 0xf, 0xf, false)));
    { auto q = __builtin_amdgcn_permlane16_swap(__float_as_uint(mx), __float_as_uint(mx), false, false); mx = fmaxf(__uint_as_float(q[0]), __uint_as_float(q[1])); }
    int ex = (int)((__float_as_uint(mx) >> 23) & 0xff);
    ex = ex < 20 ? 20 : (ex > 230 ? 230 : ex);
    const float sc = __uint_as_float((unsigned)(127 + 12 + 127 - ex) << 23);
    cb.re *= sc; cb.im *= sc;
    if (wv < 2 && (lane & 31) == 0) scl[mode >> 5] = __uint_as_float((unsigned)(ex - 12) << 23);
    auto cexp = [&](float l) {
      const float e = __builtin_amdgcn_exp2f(a2 * l), ph = __builtin_amdgcn_fractf(br * l);
      return cf{e * __builtin_amdgcn_cosf(ph), e * __builtin_amdgcn_sinf(ph)};
    };
    char* trow = smem + 0 + mode * 4;
    auto put = [&](int row, float re, float im) {
      typedef __attribute__((ext_vector_type(2))) _Float16 f16x2;
      *(f16x2*)(trow + row * KG_RS) = __builtin_convertvector(f32x2{re, im}, f16x2);
    };
    const cf w128 = cexp(128.f);
    if (wv < 2) {
      const cf w1_ = cexp(1.f), w8 = cexp(8.f), w64 = cexp(64.f);
      cf e0 = {1.f, 0.f}, e1 = w64, x = cb;
#pragma unroll
      for (int j = 0; j < 8; ++j) {
        put(j, e0.re, -e0.im); put(8 + j, e1.re, -e1.im);
        const cf y = cmul(x, w128);
        put(16 + j, x.re, x.im); put(24 + j, y.re, y.im);
        if (j < 7) { e0 = cmul(e0, w8); e1 = cmul(e1, w8); x = cmul(x, w1_); }
      }
    } else {
      const cf w2_ = cexp(2.f), w4 = cmul(w2_, w2_);
      cf x = cmul(cb, cexp(256.f));
#pragma unroll
      for (int j = 0; j < 4; ++j) {
        const cf y = cmul(x, w128);
        put(32 + j, x.re, x.im); put(36 + j, y.re, y.im);
        if (j < 3) x = cmul(x, w2_);
      }
      x = cmul(cb, cexp(512.f));
#pragma unroll
      for (int j = 0; j < 4; ++j) {
        const cf y = cmul(x, w4);
        put(40 + 2 * j, x.re, x.im); put(41 + 2 * j, y.re, y.im);
        if (j < 3) x = cmul(x, w128);
      }
      x = cmul(cb, cexp(1024.f));
      cf z = cmul(cb, cexp(1536.f));
#pragma unroll
      for (int j = 0; j < 5; ++j) {
        if (j < 4) put(48 + j, x.re, x.im);
        put(52 + j, z.re, z.im);
        if (j < 4) { x = cmul(x, w128); z = cmul(z, w128); }
      }
    }
  }
  lds_barrier();
  if (wv < 3) {
    const int arow = 16 + 16 * wv + (wv == 2 && p > 8 ? 8 : p);
    f16x8 fa[8], fb[8];
#pragma unroll
    for (int k = 0; k < 8; ++k) {
      const char* q = smem + 0 + (16 * k + 4 * g) * 4;
      fa[k] = *(const f16x8*)(q + arow * KG_RS); fb[k] = *(const f16x8*)(q + p * KG_RS);
    }
    const f32x4 sc4 = *(const f32x4*)scl;
    f32x4 acc[4] = {};
    asm volatile("" : "+v"(acc[0]), "+v"(acc[1]), "+v"(acc[2]), "+v"(acc[3]));
    __builtin_amdgcn_sched_barrier(0);
#pragma unroll
    for (int k = 0; k < 8; ++k) acc[k >> 1] = __builtin_amdgcn_mfma_f32_16x16x32_f16(fa[k], fb[k], acc[k >> 1], 0, 0, 0);
    __builtin_amdgcn_sched_barrier(0);
    const f32x4 sum = (acc[0] * sc4[0] + acc[1] * sc4[1]) + (acc[2] * sc4[2] + acc[3] * sc4[3]);
    const int b0 = wv == 0 ? 4 * (g & 1) + 128 * (g >> 1) + 8 * p : (wv == 2 ? 512 + 64 * g + p : (g < 2 ? 256 + 64 * g + 4 * p : 256 + 64 * g + 2 * p));
    const int s1 = wv == 2 ? 16 : 1, s2 = (wv == 2 || (wv == 1 && g >= 2)) ? 32 : 2;
#pragma unroll
    for (int r = 0; r < 4; ++r) ktab[min(b0 + (r & 1) * s1 + (r >> 1) * s2, 656)] = sum[r];
  }
}

#define RT0V 192
constexpr int RT0 = RT0V;
constexpr int K0_LDS = 57 * KG_RS + 16;
__global__ __launch_bounds__(256, 5) void k0_prep(const u16* x, const u16* Are, const u16* Aim, const u16* Bre, const u16* Bim,
    const u16* Cre, const u16* Cim, const u16* logdt, const u16* w1, const u16* w3, const u16* w2, const u16* c, const u16* wmod, char* ws) {
  extern __shared__ __attribute__((aligned(16))) char smem[];
  KT_BEGIN(1)
  const int b = blockIdx.x < 920 ? (blockIdx.x < 576 ? blockIdx.x + 344 : 919 - blockIdx.x) : blockIdx.x;
  if (b < 216) {
    mod_block(b, c, wmod, (float*)(ws + WS_MODP), smem);
  } else if (b < 344) {
    ln1_block(b - 216, x, (u16*)(ws + WS_XT), (float*)(ws + WS_ZT), smem);
    if (b < 218) *(u32x4*)(ws + WS_CNT + (b - 216) * 4096 + threadIdx.x * 16) = u32x4{0, 0, 0, 0};
  } else if (b < 344 + 576) {
    kgen_block(b - 344, Are, Aim, Bre, Bim, Cre, Cim, logdt, (float*)(ws + WS_KR) + (b - 344) * 768, smem);
  } else {
    const int u = b - 920;
    u32x4 r0[3];
    if (u < 144) retile1_load(u, w1, w3, r0); else retile2_load(u, w2, r0);
    if (u < 144) retile_stage<2>(r0, smem); else retile_stage<3>(r0, smem);
    __syncthreads();
    if (u < 144) retile1_emit(u, smem, ws + WS_WT); else retile2_emit(u, smem, ws + WS_WT);
  }
  KT_END(1, ws);
}

constexpr int CP_STRIDE = 4160;
constexpr int K1_U = 0;
constexpr int K1_TAB = 4608;
constexpr int K1_OUT = K1_TAB + 4 * CP_STRIDE;
constexpr int K1_ST = 4 * 2560 * 4, K1_LDS = K1_ST + 12288;
__global__ __launch_bounds__(256, 3) void k1_conv(const u16* Dskip, const u16* w1, const u16* w3, const u16* w2, char* ws) {
  extern __shared__ __attribute__((aligned(16))) char smem[];
  STAMP_DECL
  STAMP(0);
  const int d = blockIdx.x, tid = threadIdx.x, lane = tid & 63, wv = __builtin_amdgcn_readfirstlane(tid >> 6);
  const int p = lane & 15, g = lane >> 4;
  float* tab = (float*)(smem + K1_OUT);
  const float* ktab = (const float*)(ws + WS_KR) + d * 768 + tid;
  const float k0 = ktab[0], k1 = ktab[256], k2 = ktab[tid <= 128 ? 512 : 0];
  const u32x4 xv = LDP((const u32x4*)((const u16*)(ws + WS_XT) + (size_t)d * L + 8 * tid));
  const float* stat = (const float*)(ws + WS_ZT) + 8 * tid;
  const f32x4 m0 = *(const f32x4*)stat, m1 = *(const f32x4*)(stat + 4), r0 = *(const f32x4*)(stat + L), r1 = *(const f32x4*)(stat + L + 4);
  float mq = ((const float*)(ws + WS_MODP))[(lane & 7) * 3456 + min(lane >> 3, 5) * D + d];
  const float dsk = bf2f(Dskip[d]);
  __builtin_amdgcn_sched_barrier(0);
  tab[tid] = k0; tab[256 + tid] = k1;
  if (tid <= 128) tab[512 + tid] = k2;
  STAMP(1); STAMP(2);
  mq += __int_as_float(__builtin_amdgcn_update_dpp(0, __float_as_int(mq), 0xB1, 0xf, 0xf, false));
  mq += __int_as_float(__builtin_amdgcn_update_dpp(0, __float_as_int(mq), 0x4E, 0xf, 0xf, false));
  mq += __int_as_float(__builtin_amdgcn_update_dpp(0, __float_as_int(mq), 0x141, 0xf, 0xf, false));
  mq = rb(mq);
  auto bc = [&](int i) { return __int_as_float(__builtin_amdgcn_readlane(__float_as_int(mq), 8 * i)); };
  const float sa = bc(0), ca1 = rb(1.f + bc(1)), ga = bc(2);
  u32x4 hp;
#pragma unroll
  for (int e = 0; e < 4; ++e) {
    const f32x2 mm = e < 2 ? f32x2{m0[2 * e], m0[2 * e + 1]} : f32x2{m1[2 * e - 4], m1[2 * e - 3]};
    const f32x2 rr = e < 2 ? f32x2{r0[2 * e], r0[2 * e + 1]} : f32x2{r1[2 * e - 4], r1[2 * e - 3]};
    const f32x2 hh = rb2(rb2(rb2(f32x2{bflo(xv[e]), bfhi(xv[e])} - mm) * rr) * ca1) + sa;
    hp[e] = pk2(hh.x, hh.y);
  }
  *(u32x4*)(smem + K1_U + 480 + 16 * tid) = hp;
  if (tid < 30) *(u32x4*)(smem + K1_U + 16 * tid) = u32x4{0, 0, 0, 0};
  STAMP(3);
  lds_barrier();
  u32x4 rt[3];
  const bool side = (d & 255) >= 64;
  const int ru = RT0 + (d & 255) - 64 + 192 * (d >> 8);
  const bool rtl = side && ru < 576;
  if (rtl) retile2_load(ru, w2, rt);
  unsigned pf = 0;
  if (side && wv >= 4 - PFW) pf = *(const unsigned*)(((d >> 8) ? w3 : w1) + (size_t)((d & 255) - 64 + 192 * (3 - wv)) * F + 576 * (lane >> 3) + 64 * (lane & 7));
  __builtin_amdgcn_sched_barrier(0);
  {
    float kv[12];
#pragma unroll
    for (int i = 0; i < 12; i += 2) {
      const int l = 2047 - (8 * tid + i);
      const unsigned bits = pk2((float)l, (float)(l - 1));
      const int i0 = l < 128 ? (l < 0 ? 0 : l) : (int)(bits & 0xffff) - 17024, i1 = l - 1 < 128 ? (l < 1 ? 0 : l - 1) : (int)(bits >> 16) - 17024;
      kv[i] = tab[i0]; kv[i + 1] = tab[i1];
      if (i >= 8 && tid == 255) kv[i] = kv[i + 1] = 0.f;
    }
    unsigned w[6];
#pragma unroll
    for (int i = 0; i < 6; ++i) w[i] = pk2(kv[2 * i], kv[2 * i + 1]);
    auto al = [](unsigned hi, unsigned lo) { return __builtin_amdgcn_alignbit(hi, lo, 16); };
    char* cp = smem + K1_TAB + 16 * tid;
    *(u32x4*)(cp + 0 * CP_STRIDE) = u32x4{w[0], w[1], w[2], w[3]};
    *(u32x4*)(cp + 1 * CP_STRIDE) = u32x4{al(w[1], w[0]), al(w[2], w[1]), al(w[3], w[2]), al(w[4], w[3])};
    *(u32x4*)(cp + 2 * CP_STRIDE) = u32x4{w[1], w[2], w[3], w[4]};
    *(u32x4*)(cp + 3 * CP_STRIDE) = u32x4{al(w[2], w[1]), al(w[3], w[2]), al(w[4], w[3]), al(w[5], w[4])};
    if (tid < 8) *(u32x4*)(smem + K1_TAB + (tid >> 1) * CP_STRIDE + 4096 + 16 * (tid & 1)) = u32x4{0, 0, 0, 0};
  }
  STAMP(4);
  lds_barrier();
  const int sg = (3 - p) & 3;
  const unsigned abase = K1_TAB + sg * CP_STRIDE + 2 * (2031 + 8 * g - p - sg);
  const unsigned bbase = K1_U + 2 * (240 + 16 * p - 16 + 8 * g);
  f32x4 acc[8] = {};
#pragma unroll
  for (int i = 0; i < 8; ++i) asm volatile("" : "+v"(acc[i]));
#pragma unroll
  for (int cc = 0; cc < 2; ++cc) {
    const int c0 = 2 * wv + cc;
    bf16x8 A[8], B[8];
#pragma unroll
    for (int i = 0; i < 8; ++i) {
      const char* pa = smem + abase - 64 * c0 - 512 * i;
      u32x2 lo = *(const u32x2*)pa, hi = *(const u32x2*)(pa + 8);
      A[i] = __builtin_bit_cast(bf16x8, u32x4{lo.x, lo.y, hi.x, hi.y});
      B[i] = *(const bf16x8*)(smem + bbase - 64 * c0 + 512 * i);
    }
#pragma unroll
    for (int c1 = 0; c1 < 8; ++c1)
#pragma unroll
      for (int i = c1; i < 8; ++i) acc[i] = __builtin_amdgcn_mfma_f32_16x16x32_bf16(A[c1], B[i - c1], acc[i], 0, 0, 0);
  }
  float* red = (float*)smem;
  if (rtl) retile_stage<3>(rt, smem + K1_ST);
  STAMP(5);
  lds_barrier();
#pragma unroll
  for (int i = 0; i < 8; ++i) *(f32x4*)(red + wv * 2560 + (16 * i + p) * 20 + 4 * g) = acc[i];
  lds_barrier();
  STAMP(6);
  float y[8];
  {
    const int o = (tid >> 1) * 20 + (tid & 1) * 8;
#pragma unroll
    for (int hh = 0; hh < 2; ++hh) {
      f32x4 a = *(const f32x4*)(red + o + 4 * hh), b = *(const f32x4*)(red + 2560 + o + 4 * hh);
      f32x4 c = *(const f32x4*)(red + 5120 + o + 4 * hh), e = *(const f32x4*)(red + 7680 + o + 4 * hh);
      f32x4 s = (a + b) + (c + e);
#pragma unroll
      for (int k = 0; k < 4; ++k) y[4 * hh + k] = s[k];
    }
  }
  float o[8];
#pragma unroll
  for (int e = 0; e < 4; ++e) {
    const f32x2 v = f32x2{bflo(xv[e]), bfhi(xv[e])} + rb2(rb2(rb2(f32x2{y[2 * e], y[2 * e + 1]}) + rb2(f32x2{bflo(hp[e]), bfhi(hp[e])} * dsk)) * ga);
    o[2 * e] = v.x; o[2 * e + 1] = v.y;
  }
  STNT((u32x4{pk2(o[0], o[1]), pk2(o[2], o[3]), pk2(o[4], o[5]), pk2(o[6], o[7])}), (u32x4*)((u16*)(ws + WS_X1T) + (size_t)d * L + 8 * tid));
  if (rtl) retile2_emit(ru, smem + K1_ST, ws + WS_WT);
  if (tid >= 24 && tid < 48 && (tid & 7) == 0) ((u16*)(ws + WS_MODF))[((tid >> 3) - 3) * D + d] = (u16)pk2(tid == 32 ? 1.f + mq : mq, 0.f);
  asm volatile("" :: "v"(pf));
  STAMP(7);
}

constexpr int K3_H = 73728, K3_ST = 2 * 73728, K3_LDS = 2 * 73728 + 8192;
#define LDIDX(i) (i)
constexpr int RING = 16;
DEV unsigned swiglu_tile(const f32x4& c) {
  auto s0 = __builtin_amdgcn_permlane32_swap(__float_as_uint(c[0]), __float_as_uint(c[2]), false, false);
  auto s1 = __builtin_amdgcn_permlane32_swap(__float_as_uint(c[1]), __float_as_uint(c[3]), false, false);
  const f32x2 gt = {__uint_as_float(s0[0]), __uint_as_float(s1[0])}, up = {__uint_as_float(s0[1]), __uint_as_float(s1[1])};
  const f32x2 ar = gt * -1.44269504088896341f;
  const f32x2 dn = f32x2{__builtin_amdgcn_exp2f(ar.x), __builtin_amdgcn_exp2f(ar.y)} + 1.f;
  const f32x2 hv = gt * f32x2{__builtin_amdgcn_rcpf(dn.x), __builtin_amdgcn_rcpf(dn.y)} * up;
  return pk2(hv.x, hv.y);
}
DEV bf16x8 tfrag(const u32x4 (&raw)[8], int i) {
  const unsigned sel = (i & 1) ? 0x07060302u : 0x05040100u;
  u32x4 o;
#pragma unroll
  for (int q = 0; q < 4; ++q) o[q] = __builtin_amdgcn_perm(raw[2 * q + 1][i >> 1], raw[2 * q][i >> 1], sel);
  return __builtin_bit_cast(bf16x8, o);
}
template <bool ADD> DEV void arrive_wait(const char* cnt, int wv) {
  unsigned zero = 0, one = 1, tmp, sv, left;
  unsigned long long ex;
  asm volatile(
      "s_cmp_lg_u32 %[wv], 0\n\t"
      "s_cbranch_scc1 2f\n\t"
      "s_mov_b64 %[ex], exec\n\t"
      "s_mov_b64 exec, 1\n\t"
      "s_nop 1\n\t"
      ".if %c[add]\n\t"
      "global_atomic_add %[zero], %[one], %[cnt]\n\t"
      ".endif\n\t"
      "s_mov_b32 %[left], 0x100000\n\t"
      "1:\n\t"
      "global_load_dword %[tmp], %[zero], %[cnt] sc1\n\t"
      "s_waitcnt vmcnt(0)\n\t"
      "v_readfirstlane_b32 %[sv], %[tmp]\n\t"
      "s_cmp_ge_u32 %[sv], 8\n\t"
      "s_cbranch_scc1 3f\n\t"
      "s_sleep 1\n\t"
      "s_sub_u32 %[left], %[left], 1\n\t"
      "s_cmp_lg_u32 %[left], 0\n\t"
      "s_cbranch_scc1 1b\n\t"
      "3:\n\t"
      "s_mov_b64 exec, %[ex]\n\t"
      "2:\n\t"
      : [tmp] "=&v"(tmp), [sv] "=&s"(sv), [left] "=&s"(left), [ex] "=&s"(ex)
      : [wv] "s"(wv), [zero] "v"(zero), [one] "v"(one), [cnt] "s"(cnt), [add] "i"(ADD ? 1 : 0)
      : "memory", "scc");
}
DEV void raw_barrier() { asm volatile("" ::: "memory"); __builtin_amdgcn_s_barrier(); asm volatile("" ::: "memory"); }

template <int NT>
DEV void ffn_phase2(__amdgpu_buffer_rsrc_t rs, int wv, int lane, char* smem, int s, int mt, u16* out, bf16x8 (&ring)[RING], char* ws STAMP_ARG) {
  const unsigned vo = lane * 16;
  constexpr int F0 = NT == 5 ? 36 : 0, NFR = F0 + 18 * NT, NA = NT - 2;
  auto ldw = [&](int idx) { return __builtin_bit_cast(bf16x8, __builtin_amdgcn_raw_buffer_load_b128(rs, vo, idx * 1024, 0)); };
  char* pbase = ws + WS_P + (size_t)mt * 8 * 73728;
  __amdgpu_buffer_rsrc_t rp = __builtin_amdgcn_make_buffer_rsrc(pbase, 0, 8 * 73728, 0x00020000);
  const char* cnt = ws + WS_CNT + mt * 128;
  bf16x8 b[2][4];
#pragma unroll
  for (int tt = 0; tt < 4; ++tt) b[0][tt] = *(const bf16x8*)(smem + K3_H + tt * 1024 + lane * 16);
#pragma unroll
  for (int gq = 0; gq < 2; ++gq) {
    const int NG = gq ? 2 : NA, I0 = F0 + (gq ? 18 * NA : 0);
    f32x4 oc[3][4];
#pragma unroll
    for (int ks = 0; ks < 18; ++ks) {
      if (gq == 0 && ks == 17) asm volatile("" ::: "memory");
      if (gq == 0 || ks < 17) {
#pragma unroll
        for (int tt = 0; tt < 4; ++tt) b[(ks + 1) & 1][tt] = *(const bf16x8*)(smem + K3_H + ((ks + 1) % 18) * 4096 + tt * 1024 + lane * 16);
      }
      if (gq == 1 && ks == 8) {
        asm volatile("s_waitcnt vmcnt(12)" ::: "memory");
        raw_barrier();
        __builtin_amdgcn_raw_ptr_buffer_atomic_add_i32(1, __builtin_amdgcn_make_buffer_rsrc((void*)cnt, 0, 4, 0x00020000), (wv * 64 + lane) * 4, 0, 0);
      }
#pragma unroll
      for (int ti = 0; ti < NG; ++ti) {
        const int i = I0 + ks * NG + ti;
#pragma unroll
        for (int tt = 0; tt < 4; ++tt)
          oc[ti][tt] = __builtin_amdgcn_mfma_f32_16x16x32_bf16(ring[i % RING], b[ks & 1][tt], ks == 0 ? f32x4{0.f, 0.f, 0.f, 0.f} : oc[ti][tt], 0, 0, 0);
        if (i + RING < NFR) ring[i % RING] = ldw(i + RING);
        __builtin_amdgcn_sched_barrier(0);
      }
    }
    const int slot0 = gq ? 40 + 4 * wv : (NT == 5 ? 6 * wv : 24 + 4 * (wv - 4));
#pragma unroll
    for (int ti = 0; ti < NG; ++ti)
#pragma unroll
      for (int tp = 0; tp < 2; ++tp) {
        const f32x4 u = oc[ti][2 * tp], v = oc[ti][2 * tp + 1];
        __builtin_amdgcn_raw_buffer_store_b128(u32x4{pk2(u[0], u[1]), pk2(u[2], u[3]), pk2(v[0], v[1]), pk2(v[2], v[3])}, rp,
                                               vo + (s * 72 + slot0) * 1024 + (ti * 2 + tp) * 1024, 0, 16  );
      }
    __builtin_amdgcn_sched_barrier(0);
  }
  STAMP(8);
  const bool hasA = wv >= 1 && wv <= 5, hasB = wv >= 6 || wv <= 1;
  int slot[2], d0[2], t0[2];
  {
    slot[0] = 5 * s + wv - 1;
    const int ai = slot[0] >> 1, aq = (ai * 11) >> 5;
    const int dta = ai < 12 ? 5 * aq + (ai - 3 * aq) : 20 + 4 * ((ai - 12) >> 1) + (ai & 1);
    const int bs = 4 * s + ((wv + 2) & 3), bi = bs >> 1, bw = bi >> 1;
    slot[1] = 40 + bs;
    const int dtb = (bw < 4 ? 5 * bw + 3 : 4 * bw + 6) + (bi & 1);
    d0[0] = 16 * dta + 4 * (lane >> 4); d0[1] = 16 * dtb + 4 * (lane >> 4);
    t0[0] = 64 * mt + 32 * (slot[0] & 1) + (lane & 15); t0[1] = 64 * mt + 32 * (bs & 1) + (lane & 15);
  }
  const u16* x1T = (const u16*)(ws + WS_X1T);
  const u16* mf = (const u16*)(ws + WS_MODF);
  unsigned xr[2][8]; u32x2 gb[2];
#pragma unroll
  for (int k = 0; k < 2; ++k)
    if (k ? hasB : hasA) {
#pragma unroll
      for (int e = 0; e < 4; ++e) { xr[k][e] = x1T[(size_t)(d0[k] + e) * L + t0[k]]; xr[k][4 + e] = x1T[(size_t)(d0[k] + e) * L + t0[k] + 16]; }
      gb[k] = *(const u32x2*)(mf + 2 * D + d0[k]);
    }
  asm volatile("s_waitcnt vmcnt(0)" ::: "memory");
  arrive_wait<false>(cnt, wv);
  STAMP(9);
  raw_barrier();
  u32x4 pv[2][8];
  if (hasA)
#pragma unroll
    for (int q = 0; q < 8; ++q) pv[0][q] = __builtin_amdgcn_raw_buffer_load_b128(rp, vo, q * 73728 + slot[0] * 1024, 16  );
  arrive_wait<true>(cnt + 4096, wv);
  raw_barrier();
  STAMP(10);
  if (hasB)
#pragma unroll
    for (int q = 0; q < 8; ++q) pv[1][q] = __builtin_amdgcn_raw_buffer_load_b128(rp, vo, q * 73728 + slot[1] * 1024, 16  );
#pragma unroll
  for (int k = 0; k < 2; ++k)
    if (k ? hasB : hasA) {
      float o[8];
#pragma unroll
      for (int e = 0; e < 8; ++e) {
        const float a = ((elem(pv[k][0], e) + elem(pv[k][1], e)) + (elem(pv[k][2], e) + elem(pv[k][3], e))) +
                        ((elem(pv[k][4], e) + elem(pv[k][5], e)) + (elem(pv[k][6], e) + elem(pv[k][7], e)));
        const unsigned gw = gb[k][(e & 3) >> 1];
        o[e] = bf2f(xr[k][e]) + rb(((e & 1) ? bfhi(gw) : bflo(gw)) * rb(a));
      }
      *(u32x2*)(out + (size_t)t0[k] * D + d0[k]) = u32x2{pk2(o[0], o[1]), pk2(o[2], o[3])};
      *(u32x2*)(out + (size_t)(t0[k] + 16) * D + d0[k]) = u32x2{pk2(o[4], o[5]), pk2(o[6], o[7])};
    }
  STAMP(11);
  STAMP_OUT(ws, 12);
}

DEV void ffn_phase1(const char* wp, const char* whead, const char* pfp, __amdgpu_buffer_rsrc_t rs, int wv, int lane, char* smem, u32x4 (&raw)[2][8], int s, int mt, u16* out, char* ws STAMP_ARG) {
  bf16x8 ring[RING];
  const unsigned vo = lane * 16;
  auto ldw = [&](int idx) { return __builtin_bit_cast(bf16x8, __builtin_amdgcn_raw_buffer_load_b128(rs, vo, idx * 1024, 0)); };
  unsigned boff[4];
#pragma unroll
  for (int tt = 0; tt < 4; ++tt) boff[tt] = tt * 1024 + ((lane ^ ((2 * tt + ((lane >> 3) & 1)) & 7)) * 16);
  f32x4 acc[8][4] = {};
  bf16x8 b[4];
#pragma unroll
  for (int tt = 0; tt < 4; ++tt) b[tt] = *(const bf16x8*)(smem + boff[tt]);
  bf16x8 a = tfrag(raw[0], 0);
  auto ldrow = [&](int j) {
    const char* p;
    asm("v_lshl_add_u64 %0, %1, 0, %2" : "=v"(p) : "v"(wp), "s"((unsigned long long)j * (F * 2)));
    return *(const __attribute__((address_space(1))) u32x4*)p;
  };
#define SGB(m, n) __builtin_amdgcn_sched_group_barrier(m, n, 0)
  auto step = [&](int par, int ksn, unsigned inc) {
    bf16x8 hi[4];
#pragma unroll
    for (int i = 0; i < 4; ++i) {
      bf16x8 an;
      if (i < 3) an = tfrag(raw[par], i + 1);
      hi[i] = tfrag(raw[par], 4 + i);
#pragma unroll
      for (int tt = 0; tt < 4; ++tt) acc[i][tt] = __builtin_amdgcn_mfma_f32_16x16x32_bf16(a, b[tt], acc[i][tt], 0, 0, 0);
      if (i < 3) a = an;
      SGB(0x8, 1); SGB(0x2, 2); SGB(0x8, 1); SGB(0x2, 2); SGB(0x8, 1); SGB(0x2, 2); SGB(0x8, 1); SGB(0x2, 2);
      __builtin_amdgcn_sched_barrier(0);
    }
#pragma unroll
    for (int i = 4; i < 6; ++i) {
#pragma unroll
      for (int j = 0; j < 3; ++j) raw[par][3 * (i - 4) + j] = ldrow(3 * (i - 4) + j);
#pragma unroll
      for (int tt = 0; tt < 4; ++tt) acc[i][tt] = __builtin_amdgcn_mfma_f32_16x16x32_bf16(hi[i - 4], b[tt], acc[i][tt], 0, 0, 0);
      SGB(0x8, 1); SGB(0x2, 1); SGB(0x20, 1); SGB(0x8, 1); SGB(0x2, 1); SGB(0x20, 1); SGB(0x8, 1); SGB(0x2, 1); SGB(0x20, 1); SGB(0x8, 1);
      __builtin_amdgcn_sched_barrier(0);
    }
    const char* bn = smem + ksn * 4096;
#pragma unroll
    for (int tt = 0; tt < 4; ++tt) {
      if (tt < 2) raw[par][6 + tt] = ldrow(6 + tt);
      if (tt == 2) { a = tfrag(raw[par ^ 1], 0); wp += inc; }
      acc[6][tt] = __builtin_amdgcn_mfma_f32_16x16x32_bf16(hi[2], b[tt], acc[6][tt], 0, 0, 0);
      acc[7][tt] = __builtin_amdgcn_mfma_f32_16x16x32_bf16(hi[3], b[tt], acc[7][tt], 0, 0, 0);
      b[tt] = *(const bf16x8*)(bn + boff[tt]);
      SGB(0x8, 1); SGB(0x2, 3); SGB(0x20, 1); SGB(0x8, 1); SGB(0x2, 3); SGB(0x100, 1);
      __builtin_amdgcn_sched_barrier(0);
    }
  };
  unsigned pf = 0;
#pragma unroll 1
  for (int it = 0; it < 9; ++it) {
    const bool last = it == 8;
    if (last) wp = whead;
    const unsigned inc = (last ? 8 : 32) * (F * 2);
    step(0, 2 * it + 1, inc);
    step(1, last ? 0 : 2 * it + 2, inc);
  }
#pragma unroll
  for (int i = 0; i < 16; ++i) ring[i] = __builtin_bit_cast(bf16x8, raw[i >> 3][i & 7]);
  STAMP(5);
  char* hw0 = smem + K3_H + (2 * wv) * 4096 + lane * 16;
  auto put4 = [&](int h, int tt) {
    *(u32x4*)(hw0 + h * 4096 + tt * 1024) = u32x4{swiglu_tile(acc[4 * h][tt]), swiglu_tile(acc[4 * h + 1][tt]), swiglu_tile(acc[4 * h + 2][tt]), swiglu_tile(acc[4 * h + 3][tt])};
  };
  if (wv < 4) {
    put4(0, 0); put4(0, 1);
    __builtin_amdgcn_sched_barrier(0);
    f32x4 am[2][4] = {};
#pragma unroll
    for (int ks = 0; ks < 18; ++ks) {
#pragma unroll
      for (int tt = 0; tt < 4; ++tt) {
        am[0][tt] = __builtin_amdgcn_mfma_f32_16x16x32_bf16(ring[(2 * ks) % RING], b[tt], am[0][tt], 0, 0, 0);
        am[1][tt] = __builtin_amdgcn_mfma_f32_16x16x32_bf16(ring[(2 * ks + 1) % RING], b[tt], am[1][tt], 0, 0, 0);
        if (ks < 17) b[tt] = *(const bf16x8*)(smem + (ks + 1) * 4096 + boff[tt]);
      }
      ring[(2 * ks) % RING] = ldw(2 * ks + RING);
      ring[(2 * ks + 1) % RING] = ldw(2 * ks + 1 + RING);
      if (ks < 18 && ks % 3 == 0) put4((ks / 3 + 2) >> 2, (ks / 3 + 2) & 3);
      __builtin_amdgcn_sched_barrier(0);
    }
    char* hw8 = smem + K3_H + (16 + (wv >> 1)) * 4096 + lane * 16 + (wv & 1) * 8;
#pragma unroll
    for (int tt = 0; tt < 4; ++tt) *(u32x2*)(hw8 + tt * 1024) = u32x2{swiglu_tile(am[0][tt]), swiglu_tile(am[1][tt])};
    STAMP(6);
    __syncthreads();
    STAMP(7);
    ffn_phase2<5>(rs, wv, lane, smem, s, mt, out, ring, ws STAMP_PASS);
    __builtin_amdgcn_endpgm();
  }
#pragma unroll
  for (int q = 0; q < 8; ++q) put4(q >> 2, q & 3);
  STAMP(6);
  __syncthreads();
  STAMP(7);
  ffn_phase2<4>(rs, wv, lane, smem, s, mt, out, ring, ws STAMP_PASS);
  __builtin_amdgcn_endpgm();
}
__global__ __launch_bounds__(512, 2) void k3_ffn(const u16* w1, const u16* w3, char* ws, u16* out) {
  extern __shared__ __attribute__((aligned(16))) char smem[];
  const int tid = threadIdx.x, lane = tid & 63;
  const int wv = __builtin_amdgcn_readfirstlane(tid >> 6);
  STAMP_DECL
  STAMP(0);
  const int xc = blockIdx.x & 7, bi = blockIdx.x >> 3;
  const int s = (xc % (8 / SPX)) * SPX + bi % SPX, mt = (xc / (8 / SPX)) * (32 / SPX) + bi / SPX;
  const u16* x1T = (const u16*)(ws + WS_X1T) + 64 * mt;
  const u16* mf = (const u16*)(ws + WS_MODF);
  const int tc = tid & 7, cg = tid >> 3;
  u32x4 r[9];
#pragma unroll
  for (int e = 0; e < 8; ++e) r[e] = *(const u32x4*)(x1T + (size_t)(8 * cg + e) * L + 8 * tc);
  r[8] = *(const u32x4*)(x1T + (size_t)(512 + cg) * L + 8 * tc);
  const u32x4 sbv = *(const u32x4*)(mf + 8 * cg), cbv = *(const u32x4*)(mf + D + 8 * cg);
  const float sb8 = bf2f(mf[512 + cg]), cb8 = bf2f(mf[D + 512 + cg]);
  __builtin_amdgcn_sched_barrier(0);
  asm volatile("s_waitcnt vmcnt(0)" ::: "memory");
  STAMP(1);
  __builtin_amdgcn_sched_barrier(0);
  f32x2 xv[9][4];
#pragma unroll
  for (int e = 0; e < 9; ++e)
#pragma unroll
    for (int t = 0; t < 4; ++t) xv[e][t] = f32x2{bflo(r[e][t]), bfhi(r[e][t])};
  float s1, s2;
  {
    f32x2 p1[4], p2[4];
#pragma unroll
    for (int t = 0; t < 4; ++t) {
      p1[t] = xv[0][t]; p2[t] = xv[0][t] * xv[0][t];
#pragma unroll
      for (int e = 1; e < 9; ++e) { p1[t] += xv[e][t]; p2[t] += xv[e][t] * xv[e][t]; }
    }
    auto sw32 = [](float a, float b) { auto q = __builtin_amdgcn_permlane32_swap(__float_as_uint(a), __float_as_uint(b), false, false); return __uint_as_float(q[0]) + __uint_as_float(q[1]); };
    auto sw16 = [](float a, float b) { auto q = __builtin_amdgcn_permlane16_swap(__float_as_uint(a), __float_as_uint(b), false, false); return __uint_as_float(q[0]) + __uint_as_float(q[1]); };
    float u1[4], u2[4];
#pragma unroll
    for (int t = 0; t < 4; ++t) { u1[t] = sw32(p1[t >> 1][t & 1], p1[2 + (t >> 1)][t & 1]); u2[t] = sw32(p2[t >> 1][t & 1], p2[2 + (t >> 1)][t & 1]); }
    const float v10 = sw16(u1[0], u1[2]), v11 = sw16(u1[1], u1[3]), v20 = sw16(u2[0], u2[2]), v21 = sw16(u2[1], u2[3]);
    const bool b3 = lane & 8;
    auto ror8 = [](float v) { return __int_as_float(__builtin_amdgcn_update_dpp(0, __float_as_int(v), 0x128, 0xf, 0xf, false)); };
    s1 = (b3 ? v11 : v10) + ror8(b3 ? v10 : v11);
    s2 = (b3 ? v21 : v20) + ror8(b3 ? v20 : v21);
  }
  float* st = (float*)(smem + K3_ST);
  *(f32x2*)(st + (wv * 64 + 8 * tc + (lane >> 3)) * 2) = f32x2{s1, s2};
  __builtin_amdgcn_sched_barrier(0);
  char* wbase = ws + WS_WT + ((size_t)s * FR_S + wave_frag_off(wv)) * 1024;
  __amdgpu_buffer_rsrc_t rs = __builtin_amdgcn_make_buffer_rsrc(wbase, 0, 126 * 1024, 0x00020000);
  const char* wp = (const char*)((lane & 8) ? w3 : w1) + ((size_t)(8 * (lane >> 4)) * F + 576 * s + 64 * wv + 8 * (lane & 7)) * 2;
  u32x4 raw[2][8];
#pragma unroll
  for (int i = 0; i < 2; ++i) {
#pragma unroll
    for (int j = 0; j < 8; ++j) raw[i][j] = *(const u32x4*)(wp + (size_t)j * (F * 2));
    wp += (size_t)32 * (F * 2);
  }
  __builtin_amdgcn_sched_barrier(0);
  STAMP(2);
  asm volatile("s_waitcnt lgkmcnt(0)" ::: "memory");
  __builtin_amdgcn_s_barrier();
  STAMP(3);
  float* sw = (float*)(smem + K3_ST + 4096) + wv * 128;
  {
    float a = 0.f, b = 0.f;
#pragma unroll
    for (int w = 0; w < 8; ++w) { f32x2 v = *(const f32x2*)(st + (w * 64 + lane) * 2); a += v.x; b += v.y; }
    const float mean = a * (1.f / D);
    const float var = fmaxf(b * (1.f / D) - mean * mean, 0.f);
    const float rd = __builtin_amdgcn_rcpf(rb(__builtin_amdgcn_sqrtf(rb(rb(var) + 1e-5f))));
    sw[lane] = rd; sw[64 + lane] = RLN2 ? -rb(mean) : -rb(mean) * rd;
  }
  const f32x4 rd0 = *(const f32x4*)(sw + 8 * tc), rd1 = *(const f32x4*)(sw + 8 * tc + 4);
  const f32x4 nm0 = *(const f32x4*)(sw + 64 + 8 * tc), nm1 = *(const f32x4*)(sw + 64 + 8 * tc + 4);
  const f32x2 rdp[4] = {{rd0[0], rd0[1]}, {rd0[2], rd0[3]}, {rd1[0], rd1[1]}, {rd1[2], rd1[3]}};
  const f32x2 nmp[4] = {{nm0[0], nm0[1]}, {nm0[2], nm0[3]}, {nm1[0], nm1[1]}, {nm1[2], nm1[3]}};
  char* base = smem + (cg >> 2) * 4096 + (tc >> 1) * 1024 + (cg & 3) * 256 + (tc & 1) * 128;
  char* base8 = smem + (16 + (wv >> 2)) * 4096 + (tc >> 1) * 1024 + (wv & 3) * 256 + (tc & 1) * 128 + (cg & 7) * 2;
#pragma unroll
  for (int t = 0; t < 4; ++t) {
    f32x2 hv[9];
#pragma unroll
    for (int e = 0; e < 9; ++e) {
      const float cb = e < 8 ? elem(cbv, e) : cb8, sb = e < 8 ? elem(sbv, e) : sb8;
      hv[e] = __builtin_elementwise_fma(__builtin_elementwise_fma(xv[e][t], rdp[t], nmp[t]), f32x2{cb, cb}, f32x2{sb, sb});
    }
#pragma unroll
    for (int p = 0; p < 2; ++p) {
      *(u32x4*)(base + (((2 * t + p) ^ tc) * 16)) = u32x4{pk2(hv[0][p], hv[1][p]), pk2(hv[2][p], hv[3][p]), pk2(hv[4][p], hv[5][p]), pk2(hv[6][p], hv[7][p])};
      *(u16*)(base8 + (((2 * t + p) ^ tc) * 16)) = (u16)pk2(hv[8][p], 0.f);
    }
  }
  asm volatile("s_waitcnt lgkmcnt(0)" ::: "memory");
  __builtin_amdgcn_s_barrier();
  STAMP(4);
  const char* pfp = (const char*)((lane & 8) ? w3 : w1) + ((size_t)(192 + 4 * (bi / SPX) + (lane >> 4)) * F + 576 * s + 64 * (lane & 7)) * 2;
  ffn_phase1(wp, ws + WS_WT + ((size_t)s * FR_S + wv) * 1024 + lane * 16, pfp, rs, wv, lane, smem, raw, s, mt, out, ws STAMP_PASS);
}

extern "C" void kernel_launch(void* const* d_in, const int* in_sizes, int n_in, void* d_out, int out_size, void* d_ws,
                              size_t ws_size, hipStream_t stream) {
  const u16* x = (const u16*)d_in[0];
  const u16 *Are = (const u16*)d_in[1], *Aim = (const u16*)d_in[2], *Bre = (const u16*)d_in[3], *Bim = (const u16*)d_in[4];
  const u16 *Cre = (const u16*)d_in[5], *Cim = (const u16*)d_in[6], *logdt = (const u16*)d_in[7], *Dskip = (const u16*)d_in[8];
  const u16 *w1 = (const u16*)d_in[10], *w3 = (const u16*)d_in[11], *w2 = (const u16*)d_in[12];
  const u16 *c = (const u16*)d_in[13], *wmod = (const u16*)d_in[14];
  char* ws = (char*)d_ws;
  (void)hipFuncSetAttribute((const void*)k3_ffn, hipFuncAttributeMaxDynamicSharedMemorySize, K3_LDS);
  k0_prep<<<920 + RT0, 256, K0_LDS, stream>>>(x, Are, Aim, Bre, Bim, Cre, Cim, logdt, w1, w3, w2, c, wmod, ws);
  k1_conv<<<D, 256, K1_LDS, stream>>>(Dskip, w1, w3, w2, ws);
  k3_ffn<<<256, 512, K3_LDS, stream>>>(w1, w3, ws, (u16*)d_out);
}
```

```cpp
#include <hip/hip_runtime.h>
#include <hip/hip_bf16.h>
#include <stdint.h>

typedef __attribute__((ext_vector_type(8))) __bf16 bf16x8;
typedef __attribute__((ext_vector_type(2))) __bf16 bf16x2;
typedef __attribute__((ext_vector_type(8))) _Float16 f16x8;
typedef __attribute__((ext_vector_type(4))) float f32x4;
typedef __attribute__((ext_vector_type(2))) float f32x2;
typedef __attribute__((ext_vector_type(4))) unsigned u32x4;
typedef __attribute__((ext_vector_type(2))) unsigned u32x2;
typedef __attribute__((ext_vector_type(4))) short s16x4;
typedef unsigned short u16;
#define LDSP __attribute__((address_space(3)))
#define DEV __device__ __forceinline__
constexpr int PFW = 3;
constexpr int SPX = 4;
constexpr int L = 2048, D = 576, NM = 128, F = 4608;
constexpr int KR_STRIDE = 2064;
constexpr int MODK = 8;
constexpr int FR_S = 144 + 664;

constexpr size_t WS_MODP = 0;
constexpr size_t WS_MODF = 114688;
constexpr size_t WS_CNT  = 118784;
constexpr size_t WS_XT   = 131072;
constexpr size_t WS_ZT   = WS_XT + (size_t)D * L * 2;
constexpr size_t WS_X1T  = WS_ZT + (size_t)D * L * 2;
constexpr size_t WS_KR   = WS_X1T + (size_t)D * L * 2;
constexpr size_t WS_WT   = (WS_KR + (size_t)D * KR_STRIDE * 2 + 4095) / 4096 * 4096;
constexpr size_t WS_P    = WS_WT + (size_t)8 * FR_S * 1024;

DEV float bf2f(unsigned v) { return __uint_as_float(v << 16); }
DEV float bflo(unsigned v) { return __uint_as_float(v << 16); }
DEV float bfhi(unsigned v) { return __uint_as_float(v & 0xffff0000u); }
DEV float rb(float x) { return (float)(__bf16)x; }
DEV f32x2 rb2(f32x2 x) { const f32x2 p = x * 65537.f; return __builtin_elementwise_fma(x, f32x2{-65536.f, -65536.f}, p); }
DEV unsigned pk2(float a, float b) { f32x2 v = {a, b}; bf16x2 r = __builtin_convertvector(v, bf16x2); return __builtin_bit_cast(unsigned, r); }
#define LDNT(p) (*(p))
DEV float elem(const u32x4& v, int i) { unsigned w = v[i >> 1]; return (i & 1) ? bfhi(w) : bflo(w); }
DEV int wave_frag_off(int w) { return 144 - 16 + (w < 4 ? 110 * w : 440 + 56 * (w - 4)); }
DEV int frag_pos(int s, int w, int i) { return s * FR_S + (i < 16 ? i * 9 + w : wave_frag_off(w) + i); }

DEV void lds_barrier() { asm volatile("s_waitcnt lgkmcnt(0)" ::: "memory"); __builtin_amdgcn_s_barrier(); asm volatile("" ::: "memory"); }
DEV void retile1_load(int u, const u16* w1, const u16* w3, u32x4 (&r)[3]) {
  const int tid = threadIdx.x & 255, s = u & 7, ks = u >> 3;
  const size_t o = (size_t)(32 * ks + (tid >> 3)) * F + 576 * s + 512 + (tid & 7) * 8;
  r[0] = LDNT((const u32x4*)(w1 + o));
  r[1] = LDNT((const u32x4*)(w3 + o));
}
DEV void retile2_load(int u, const u16* w2, u32x4 (&r)[3]) {
  const int tid = threadIdx.x & 255, s = u & 7, v = (u >> 3) - 18, ks = (v * 43) >> 7, th = v - 3 * ks;
  const int kk = tid >> 3, gg = kk >> 3, jj = kk & 7, cp = 4 * (gg & 1) + 2 * (gg >> 1) + (jj & 1);
  const bool nrm = ks < 16;
  const int f = (nrm ? 64 * (ks >> 1) + 4 * (ks & 1) : 32 * ks) + (nrm ? 8 * cp + (jj >> 1) : cp + 8 * (jj >> 1));
  const u16* p = w2 + (size_t)(576 * s + f) * D + 192 * th + (tid & 7) * 8;
#pragma unroll
  for (int i = 0; i < 3; ++i) r[i] = LDNT((const u32x4*)(p + 64 * i));
}
template <int N> DEV void retile_stage(const u32x4 (&r)[3], char* st) {
  const int tid = threadIdx.x & 255;
  if (N == 2) { *(u32x4*)(st + tid * 16) = r[0]; *(u32x4*)(st + 4096 + tid * 16) = r[1]; }
  else {
#pragma unroll
    for (int i = 0; i < 3; ++i) *(u32x4*)(st + (tid >> 3) * 384 + (tid & 7) * 16 + 128 * i) = r[i];
  }
}
template <int RS> DEV u32x4 tr_frag(const char* p) {
  s16x4 lo = __builtin_amdgcn_ds_read_tr16_b64_v4i16((LDSP s16x4*)p);
  s16x4 hi = __builtin_amdgcn_ds_read_tr16_b64_v4i16((LDSP s16x4*)(p + 4 * RS));
  u32x2 l2 = __builtin_bit_cast(u32x2, lo), h2 = __builtin_bit_cast(u32x2, hi);
  return u32x4{l2.x, l2.y, h2.x, h2.y};
}
DEV void retile1_emit(int u, const char* st, char* wt) {
  const int lane = threadIdx.x & 63, wv = __builtin_amdgcn_readfirstlane((threadIdx.x >> 6) & 3);
  const int s = u & 7, ks = u >> 3;
  const int g = lane >> 4, qq = (lane & 15) >> 2, pp = lane & 3;
  const char* p = st + (pp >> 1) * 4096 + (8 * g + qq) * 128 + (pp & 1) * 8 + wv * 32;
#pragma unroll
  for (int i = 0; i < 2; ++i)
    *(u32x4*)(wt + (size_t)frag_pos(s, wv, 2 * ks + i) * 1024 + lane * 16) = tr_frag<128>(p + i * 16);
}
DEV void retile2_emit(int u, const char* st, char* wt) {
  const int lane = threadIdx.x & 63, wv = __builtin_amdgcn_readfirstlane((threadIdx.x >> 6) & 3);
  const int s = u & 7, v = (u >> 3) - 18, ks = (v * 43) >> 7, th = v - 3 * ks;
  const int g = lane >> 4, qq = (lane & 15) >> 2, pp = lane & 3;
  const char* p = st + (8 * g + qq) * 384 + pp * 8 + wv * 96;
#pragma unroll
  for (int i = 0; i < 3; ++i) {
    const int dt = 12 * th + wv * 3 + i;
    const int wa = (dt * 13) >> 6, ta = dt - 5 * wa, pa = 128 + 110 * wa + 36 + (ta < 3 ? ks * 3 + ta : 54 + ks * 2 + ta - 3);
    const int wb = (dt - 20) >> 2, tb = (dt - 20) & 3, ib = (tb >> 1) * 36 + ks * 2 + (tb & 1);
    const int pb = ib < 16 ? ib * 9 + 4 + wb : 128 + 440 + 56 * wb + ib;
    const int pos = __builtin_amdgcn_readfirstlane(s * FR_S + (dt < 20 ? pa : pb));
    *(u32x4*)(wt + (size_t)pos * 1024 + lane * 16) = tr_frag<384>(p + i * 32);
  }
}

struct cf { float re, im; };
DEV cf cmul(cf a, cf b) {
  cf r;
  asm("v_mul_f32 %0, %2, %4\n\tv_mul_f32 %1, %2, %5\n\tv_fma_f32 %0, -%3, %5, %0\n\tv_fma_f32 %1, %3, %4, %1"
      : "=&v"(r.re), "=&v"(r.im) : "v"(a.re), "v"(a.im), "v"(b.re), "v"(b.im));
  return r;
}
constexpr int LN_RS = 1152 + 16;
DEV void ln1_block(int u, const u16* x, u16* xT, float* stat, char* smem) {
  const int tid = threadIdx.x & 255, tok = tid >> 4, c16 = tid & 15, lane = tid & 63, wv = __builtin_amdgcn_readfirstlane(tid >> 6);
  const int t0 = 16 * ((u & 7) * 16 + (u >> 3));
  const u16* xr = x + (size_t)(t0 + tok) * D + c16 * 8;
  u32x4 r[5];
#pragma unroll
  for (int i = 0; i < 4; ++i) r[i] = LDNT((const u32x4*)(xr + 128 * i));
  r[4] = c16 < 8 ? LDNT((const u32x4*)(xr + 512)) : u32x4{0, 0, 0, 0};
#pragma unroll
  for (int i = 0; i < 4; ++i) *(u32x4*)(smem + tok * LN_RS + (c16 + 16 * i) * 16) = r[i];
  if (c16 < 8) *(u32x4*)(smem + tok * LN_RS + (c16 + 64) * 16) = r[4];
  f32x2 sp = {0.f, 0.f};
#pragma unroll
  for (int i = 0; i < 5; ++i)
#pragma unroll
    for (int e = 0; e < 4; ++e) sp += f32x2{bflo(r[i][e]), bfhi(r[i][e])};
  auto sum16 = [](float v) {
    v += __int_as_float(__builtin_amdgcn_update_dpp(0, __float_as_int(v), 0xB1, 0xf, 0xf, false));
    v += __int_as_float(__builtin_amdgcn_update_dpp(0, __float_as_int(v), 0x4E, 0xf, 0xf, false));
    v += __int_as_float(__builtin_amdgcn_update_dpp(0, __float_as_int(v), 0x124, 0xf, 0xf, false));
    v += __int_as_float(__builtin_amdgcn_update_dpp(0, __float_as_int(v), 0x128, 0xf, 0xf, false));
    return v;
  };
  const float mean = sum16(sp.x + sp.y) * (1.f / D);
  f32x2 qp = {0.f, 0.f};
#pragma unroll
  for (int i = 0; i < 5; ++i)
#pragma unroll
    for (int e = 0; e < 4; ++e) {
      f32x2 dl = f32x2{bflo(r[i][e]), bfhi(r[i][e])} - mean;
      if (i == 4 && c16 >= 8) dl = f32x2{0.f, 0.f};
      qp += dl * dl;
    }
  const float q = sum16(qp.x + qp.y);
  if (c16 == 0) { stat[t0 + tok] = rb(mean); stat[L + t0 + tok] = 1.f / rb(sqrtf(rb(rb(q * (1.f / D)) + 1e-5f))); }
  __syncthreads();
  const int g = lane >> 4, qq = (lane & 15) >> 2, pp = lane & 3;
  const char* p = smem + (8 * (g & 1) + qq) * LN_RS + pp * 8 + (g >> 1) * 32 + wv * 64;
  u16* o = xT + (size_t)(32 * wv + 16 * (g >> 1) + (lane & 15)) * L + t0 + 8 * (g & 1);
#pragma unroll
  for (int i = 0; i < 5; ++i)
    if (i < 4 || wv < 2) *(u32x4*)(o + (size_t)i * 128 * L) = tr_frag<LN_RS>(p + i * 256);
}

DEV void mod_block(int b, const u16* c, const u16* wmod, float* modp, char* smem) {
  const int tid = threadIdx.x & 255, c16 = tid & 15, rg = tid >> 4, lane = tid & 63, wv = tid >> 6;
  const int cg = b % 27, kq = b / 27;
  u32x4 r[5]; unsigned cv[5];
#pragma unroll
  for (int i = 0; i < 5; ++i) {
    const int k = 72 * kq + (i < 4 || rg < 8 ? rg + 16 * i : 0);
    r[i] = LDNT((const u32x4*)(wmod + (size_t)k * 3456 + 128 * cg + 8 * c16));
    cv[i] = c[k];
  }
  float acc[8] = {};
#pragma unroll
  for (int i = 0; i < 5; ++i) {
    const float x = bf2f(cv[i]);
    const float sv = i < 4 || rg < 8 ? rb(x / (1.f + __expf(-x))) : 0.f;
#pragma unroll
    for (int e = 0; e < 8; ++e) acc[e] += sv * elem(r[i], e);
  }
#pragma unroll
  for (int e = 0; e < 8; ++e) { acc[e] += __shfl_xor(acc[e], 16); acc[e] += __shfl_xor(acc[e], 32); }
  float* red = (float*)smem;
  if (lane < 16)
#pragma unroll
    for (int e = 0; e < 8; ++e) red[wv * 128 + lane * 8 + e] = acc[e];
  __syncthreads();
  if (tid < 128) modp[kq * 3456 + 128 * cg + tid] = (red[tid] + red[128 + tid]) + (red[256 + tid] + red[384 + tid]);
}

constexpr int KG_RS = 528;
DEV void kgen_block(int d, const u16* Are, const u16* Aim, const u16* Bre, const u16* Bim, const u16* Cre, const u16* Cim,
                    const u16* logdt, u16* kr, char* smem) {
  const int tid = threadIdx.x, lane = tid & 63, wv = __builtin_amdgcn_readfirstlane(tid >> 6), p = lane & 15, g = lane >> 4;
  float* scl = (float*)(smem + 57 * KG_RS);
  float* ktab = scl + 4;
  const int mode = tid & 127, n = d * NM + mode;
  const unsigned iare = LDNT(Are + n), iaim = LDNT(Aim + n), ibre = LDNT(Bre + n), ibim = LDNT(Bim + n), icre = LDNT(Cre + n), icim = LDNT(Cim + n), ildt = logdt[d];
  {
    const float dt = rb(expf(bf2f(ildt)));
    const float a2 = dt * bf2f(iare) * 1.44269504088896341f, br = dt * bf2f(iaim) * 0.15915494309189535f;
    cf cb = cmul(cf{bf2f(icre), bf2f(icim)}, cf{dt * bf2f(ibre), dt * bf2f(ibim)});
    float mx = fmaxf(fabsf(cb.re), fabsf(cb.im));
    mx = fmaxf(mx, __int_as_float(__builtin_amdgcn_update_dpp(0, __float_as_int(mx), 0xB1, 0xf, 0xf, false)));
    mx = fmaxf(mx, __int_as_float(__builtin_amdgcn_update_dpp(0, __float_as_int(mx), 0x4E, 0xf, 0xf, false)));
    mx = fmaxf(mx, __int_as_float(__builtin_amdgcn_update_dpp(0, __float_as_int(mx), 0x124, 0xf, 0xf, false)));
    mx = fmaxf(mx, __int_as_float(__builtin_amdgcn_update_dpp(0, __float_as_int(mx), 0x128, 0xf, 0xf, false)));
    { auto q = __builtin_amdgcn_permlane16_swap(__float_as_uint(mx), __float_as_uint(mx), false, false); mx = fmaxf(__uint_as_float(q[0]), __uint_as_float(q[1])); }
    int ex = (int)((__float_as_uint(mx) >> 23) & 0xff);
    ex = ex < 20 ? 20 : (ex > 230 ? 230 : ex);
    const float sc = __uint_as_float((unsigned)(127 + 12 + 127 - ex) << 23);
    cb.re *= sc; cb.im *= sc;
    if (wv < 2 && (lane & 31) == 0) scl[mode >> 5] = __uint_as_float((unsigned)(ex - 12) << 23);
    auto cexp = [&](float l) {
      const float e = __builtin_amdgcn_exp2f(a2 * l), ph = __builtin_amdgcn_fractf(br * l);
      return cf{e * __builtin_amdgcn_cosf(ph), e * __builtin_amdgcn_sinf(ph)};
    };
    char* trow = smem + 0 + mode * 4;
    auto put = [&](int row, float re, float im) {
      typedef __attribute__((ext_vector_type(2))) _Float16 f16x2;
      *(f16x2*)(trow + row * KG_RS) = __builtin_convertvector(f32x2{re, im}, f16x2);
    };
    const cf w128 = cexp(128.f);
    if (wv < 2) {
      const cf w1_ = cexp(1.f), w8 = cexp(8.f), w64 = cexp(64.f);
      cf e0 = {1.f, 0.f}, e1 = w64, x = cb;
#pragma unroll
      for (int j = 0; j < 8; ++j) {
        put(j, e0.re, -e0.im); put(8 + j, e1.re, -e1.im);
        const cf y = cmul(x, w128);
        put(16 + j, x.re, x.im); put(24 + j, y.re, y.im);
        if (j < 7) { e0 = cmul(e0, w8); e1 = cmul(e1, w8); x = cmul(x, w1_); }
      }
    } else {
      const cf w2_ = cexp(2.f), w4 = cmul(w2_, w2_);
      cf x = cmul(cb, cexp(256.f));
#pragma unroll
      for (int j = 0; j < 4; ++j) {
        const cf y = cmul(x, w128);
        put(32 + j, x.re, x.im); put(36 + j, y.re, y.im);
        if (j < 3) x = cmul(x, w2_);
      }
      x = cmul(cb, cexp(512.f));
#pragma unroll
      for (int j = 0; j < 4; ++j) {
        const cf y = cmul(x, w4);
        put(40 + 2 * j, x.re, x.im); put(41 + 2 * j, y.re, y.im);
        if (j < 3) x = cmul(x, w128);
      }
      x = cmul(cb, cexp(1024.f));
      cf z = cmul(cb, cexp(1536.f));
#pragma unroll
      for (int j = 0; j < 5; ++j) {
        if (j < 4) put(48 + j, x.re, x.im);
        put(52 + j, z.re, z.im);
        if (j < 4) { x = cmul(x, w128); z = cmul(z, w128); }
      }
    }
  }
  lds_barrier();
  if (wv < 3) {
    const int arow = 16 + 16 * wv + (wv == 2 && p > 8 ? 8 : p);
    f16x8 fa[8], fb[8];
#pragma unroll
    for (int k = 0; k < 8; ++k) {
      const char* q = smem + 0 + (16 * k + 4 * g) * 4;
      fa[k] = *(const f16x8*)(q + arow * KG_RS); fb[k] = *(const f16x8*)(q + p * KG_RS);
    }
    const f32x4 sc4 = *(const f32x4*)scl;
    f32x4 acc[4] = {};
    asm volatile("" : "+v"(acc[0]), "+v"(acc[1]), "+v"(acc[2]), "+v"(acc[3]));
    __builtin_amdgcn_sched_barrier(0);
#pragma unroll
    for (int k = 0; k < 8; ++k) acc[k >> 1] = __builtin_amdgcn_mfma_f32_16x16x32_f16(fa[k], fb[k], acc[k >> 1], 0, 0, 0);
    __builtin_amdgcn_sched_barrier(0);
    const f32x4 sum = (acc[0] * sc4[0] + acc[1] * sc4[1]) + (acc[2] * sc4[2] + acc[3] * sc4[3]);
    const int b0 = wv == 0 ? 4 * (g & 1) + 128 * (g >> 1) + 8 * p : (wv == 2 ? 512 + 64 * g + p : (g < 2 ? 256 + 64 * g + 4 * p : 256 + 64 * g + 2 * p));
    const int s1 = wv == 2 ? 16 : 1, s2 = (wv == 2 || (wv == 1 && g >= 2)) ? 32 : 2;
#pragma unroll
    for (int r = 0; r < 4; ++r) ktab[min(b0 + (r & 1) * s1 + (r >> 1) * s2, 656)] = sum[r];
  }
  lds_barrier();
  float kv[8];
#pragma unroll
  for (int i = 0; i < 8; i += 2) {
    const int l = 2047 - (8 * tid + i);
    const unsigned bits = pk2((float)l, (float)(l - 1));
    kv[i] = ktab[l < 128 ? l : (int)(bits & 0xffff) - 17024];
    kv[i + 1] = ktab[l - 1 < 128 ? l - 1 : (int)(bits >> 16) - 17024];
  }
  *(u32x4*)(kr + 8 * tid) = u32x4{pk2(kv[0], kv[1]), pk2(kv[2], kv[3]), pk2(kv[4], kv[5]), pk2(kv[6], kv[7])};
  if (tid < 2) *(u32x4*)(kr + 2048 + 8 * tid) = u32x4{0, 0, 0, 0};
}

constexpr int RT0 = 192;
constexpr int K0_LDS = 57 * KG_RS + 16 + 660 * 4;
__global__ __launch_bounds__(256, 5) void k0_prep(const u16* x, const u16* Are, const u16* Aim, const u16* Bre, const u16* Bim,
    const u16* Cre, const u16* Cim, const u16* logdt, const u16* w1, const u16* w3, const u16* w2, const u16* c, const u16* wmod, char* ws) {
  extern __shared__ __attribute__((aligned(16))) char smem[];
  const int b = blockIdx.x < 920 ? (blockIdx.x < 576 ? blockIdx.x + 344 : 919 - blockIdx.x) : blockIdx.x;
  if (b < 216) {
    mod_block(b, c, wmod, (float*)(ws + WS_MODP), smem);
  } else if (b < 344) {
    ln1_block(b - 216, x, (u16*)(ws + WS_XT), (float*)(ws + WS_ZT), smem);
    if (b < 218) *(u32x4*)(ws + WS_CNT + (b - 216) * 4096 + threadIdx.x * 16) = u32x4{0, 0, 0, 0};
  } else if (b < 344 + 576) {
    kgen_block(b - 344, Are, Aim, Bre, Bim, Cre, Cim, logdt, (u16*)(ws + WS_KR) + (size_t)(b - 344) * KR_STRIDE, smem);
  } else {
    const int u = b - 920;
    u32x4 r0[3];
    if (u < 144) retile1_load(u, w1, w3, r0); else retile2_load(u, w2, r0);
    if (u < 144) retile_stage<2>(r0, smem); else retile_stage<3>(r0, smem);
    __syncthreads();
    if (u < 144) retile1_emit(u, smem, ws + WS_WT); else retile2_emit(u, smem, ws + WS_WT);
  }
}

constexpr int CP_STRIDE = 4160;
constexpr int K1_U = 0;
constexpr int K1_TAB = 4608;
constexpr int K1_ST = 4 * 2560 * 4, K1_LDS = K1_ST + 12288;
__global__ __launch_bounds__(256, 3) void k1_conv(const u16* Dskip, const u16* w1, const u16* w3, const u16* w2, char* ws) {
  extern __shared__ __attribute__((aligned(16))) char smem[];
  const int d = blockIdx.x, tid = threadIdx.x, lane = tid & 63, wv = __builtin_amdgcn_readfirstlane(tid >> 6);
  const int p = lane & 15, g = lane >> 4;
  const u16* kr = (const u16*)(ws + WS_KR) + (size_t)d * KR_STRIDE + 8 * tid;
  const u32x4 k0 = LDNT((const u32x4*)kr);
  const u32x2 k1 = LDNT((const u32x2*)(kr + 8));
  const u32x4 xv = LDNT((const u32x4*)((const u16*)(ws + WS_XT) + (size_t)d * L + 8 * tid));
  const float* stat = (const float*)(ws + WS_ZT) + 8 * tid;
  const f32x4 m0 = *(const f32x4*)stat, m1 = *(const f32x4*)(stat + 4), r0 = *(const f32x4*)(stat + L), r1 = *(const f32x4*)(stat + L + 4);
  float mq = ((const float*)(ws + WS_MODP))[(lane & 7) * 3456 + min(lane >> 3, 5) * D + d];
  const float dsk = bf2f(Dskip[d]);
  __builtin_amdgcn_sched_barrier(0);
  mq += __int_as_float(__builtin_amdgcn_update_dpp(0, __float_as_int(mq), 0xB1, 0xf, 0xf, false));
  mq += __int_as_float(__builtin_amdgcn_update_dpp(0, __float_as_int(mq), 0x4E, 0xf, 0xf, false));
  mq += __int_as_float(__builtin_amdgcn_update_dpp(0, __float_as_int(mq), 0x141, 0xf, 0xf, false));
  mq = rb(mq);
  auto bc = [&](int i) { return __int_as_float(__builtin_amdgcn_readlane(__float_as_int(mq), 8 * i)); };
  const float sa = bc(0), ca1 = rb(1.f + bc(1)), ga = bc(2);
  u32x4 hp;
#pragma unroll
  for (int e = 0; e < 4; ++e) {
    const f32x2 mm = e < 2 ? f32x2{m0[2 * e], m0[2 * e + 1]} : f32x2{m1[2 * e - 4], m1[2 * e - 3]};
    const f32x2 rr = e < 2 ? f32x2{r0[2 * e], r0[2 * e + 1]} : f32x2{r1[2 * e - 4], r1[2 * e - 3]};
    const f32x2 hh = rb2(rb2(rb2(f32x2{bflo(xv[e]), bfhi(xv[e])} - mm) * rr) * ca1) + sa;
    hp[e] = pk2(hh.x, hh.y);
  }
  *(u32x4*)(smem + K1_U + 480 + 16 * tid) = hp;
  if (tid < 30) *(u32x4*)(smem + K1_U + 16 * tid) = u32x4{0, 0, 0, 0};
  {
    const unsigned w[6] = {k0.x, k0.y, k0.z, k0.w, k1.x, k1.y};
    auto al = [](unsigned hi, unsigned lo) { return __builtin_amdgcn_alignbit(hi, lo, 16); };
    char* cp = smem + K1_TAB + 16 * tid;
    *(u32x4*)(cp + 0 * CP_STRIDE) = u32x4{w[0], w[1], w[2], w[3]};
    *(u32x4*)(cp + 1 * CP_STRIDE) = u32x4{al(w[1], w[0]), al(w[2], w[1]), al(w[3], w[2]), al(w[4], w[3])};
    *(u32x4*)(cp + 2 * CP_STRIDE) = u32x4{w[1], w[2], w[3], w[4]};
    *(u32x4*)(cp + 3 * CP_STRIDE) = u32x4{al(w[2], w[1]), al(w[3], w[2]), al(w[4], w[3]), al(w[5], w[4])};
    if (tid < 8) *(u32x4*)(smem + K1_TAB + (tid >> 1) * CP_STRIDE + 4096 + 16 * (tid & 1)) = u32x4{0, 0, 0, 0};
  }
  lds_barrier();
  u32x4 rt[3];
  const bool side = (d & 255) >= 64;
  const int ru = RT0 + (d & 255) - 64 + 192 * (d >> 8);
  const bool rtl = side && ru < 576;
  if (rtl) retile2_load(ru, w2, rt);
  unsigned pf = 0;
  if (side && wv >= 4 - PFW) pf = *(const unsigned*)(((d >> 8) ? w3 : w1) + (size_t)((d & 255) - 64 + 192 * (3 - wv)) * F + 576 * (lane >> 3) + 64 * (lane & 7));
  __builtin_amdgcn_sched_barrier(0);
  const int sg = (3 - p) & 3;
  const unsigned abase = K1_TAB + sg * CP_STRIDE + 2 * (2031 + 8 * g - p - sg);
  const unsigned bbase = K1_U + 2 * (240 + 16 * p - 16 + 8 * g);
  f32x4 acc[8] = {};
#pragma unroll
  for (int i = 0; i < 8; ++i) asm volatile("" : "+v"(acc[i]));
#pragma unroll
  for (int cc = 0; cc < 2; ++cc) {
    const int c0 = 2 * wv + cc;
    bf16x8 A[8], B[8];
#pragma unroll
    for (int i = 0; i < 8; ++i) {
      const char* pa = smem + abase - 64 * c0 - 512 * i;
      const u32x2 lo = *(const volatile LDSP u32x2*)pa, hi = *(const volatile LDSP u32x2*)(pa + 8);
      A[i] = __builtin_bit_cast(bf16x8, u32x4{lo.x, lo.y, hi.x, hi.y});
      B[i] = *(const bf16x8*)(smem + bbase - 64 * c0 + 512 * i);
    }
#pragma unroll
    for (int c1 = 0; c1 < 8; ++c1)
#pragma unroll
      for (int i = c1; i < 8; ++i) acc[i] = __builtin_amdgcn_mfma_f32_16x16x32_bf16(A[c1], B[i - c1], acc[i], 0, 0, 0);
  }
  float* red = (float*)smem;
  lds_barrier();
#pragma unroll
  for (int i = 0; i < 8; ++i) *(f32x4*)(red + wv * 2560 + (16 * i + p) * 20 + 4 * g) = acc[i];
  if (rtl) retile_stage<3>(rt, smem + K1_ST);
  lds_barrier();
  float y[8];
  {
    const int o = (tid >> 1) * 20 + (tid & 1) * 8;
#pragma unroll
    for (int hh = 0; hh < 2; ++hh) {
      f32x4 a = *(const f32x4*)(red + o + 4 * hh), b = *(const f32x4*)(red + 2560 + o + 4 * hh);
      f32x4 c = *(const f32x4*)(red + 5120 + o + 4 * hh), e = *(const f32x4*)(red + 7680 + o + 4 * hh);
      f32x4 s = (a + b) + (c + e);
#pragma unroll
      for (int k = 0; k < 4; ++k) y[4 * hh + k] = s[k];
    }
  }
  float o[8];
#pragma unroll
  for (int e = 0; e < 4; ++e) {
    const f32x2 v = f32x2{bflo(xv[e]), bfhi(xv[e])} + rb2(rb2(rb2(f32x2{y[2 * e], y[2 * e + 1]}) + rb2(f32x2{bflo(hp[e]), bfhi(hp[e])} * dsk)) * ga);
    o[2 * e] = v.x; o[2 * e + 1] = v.y;
  }
  *(u32x4*)((u16*)(ws + WS_X1T) + (tid >> 3) * (D * 64) + d * 64 + (tid & 7) * 8) = u32x4{pk2(o[0], o[1]), pk2(o[2], o[3]), pk2(o[4], o[5]), pk2(o[6], o[7])};
  if (rtl) retile2_emit(ru, smem + K1_ST, ws + WS_WT);
  if (tid >= 24 && tid < 48 && (tid & 7) == 0) ((u16*)(ws + WS_MODF))[((tid >> 3) - 3) * D + d] = (u16)pk2(tid == 32 ? 1.f + mq : mq, 0.f);
  asm volatile("" :: "v"(pf));
}

constexpr int K3_H = 73728, K3_ST = 2 * 73728, K3_LDS = 2 * 73728 + 8192;
constexpr int RING = 16;
constexpr int RING2 = 32;
DEV unsigned swiglu_tile(const f32x4& c) {
  auto s0 = __builtin_amdgcn_permlane32_swap(__float_as_uint(c[0]), __float_as_uint(c[2]), false, false);
  auto s1 = __builtin_amdgcn_permlane32_swap(__float_as_uint(c[1]), __float_as_uint(c[3]), false, false);
  const f32x2 gt = {__uint_as_float(s0[0]), __uint_as_float(s1[0])}, up = {__uint_as_float(s0[1]), __uint_as_float(s1[1])};
  const f32x2 ar = gt * -1.44269504088896341f;
  const f32x2 dn = f32x2{__builtin_amdgcn_exp2f(ar.x), __builtin_amdgcn_exp2f(ar.y)} + 1.f;
  const f32x2 hv = gt * f32x2{__builtin_amdgcn_rcpf(dn.x), __builtin_amdgcn_rcpf(dn.y)} * up;
  return pk2(hv.x, hv.y);
}
DEV bf16x8 tfrag(const u32x4 (&raw)[8], int i) {
  const unsigned sel = (i & 1) ? 0x07060302u : 0x05040100u;
  u32x4 o;
#pragma unroll
  for (int q = 0; q < 4; ++q) o[q] = __builtin_amdgcn_perm(raw[2 * q + 1][i >> 1], raw[2 * q][i >> 1], sel);
  return __builtin_bit_cast(bf16x8, o);
}
template <bool ADD> DEV void arrive_wait(const char* cnt, int wv) {
  unsigned zero = 0, one = 1, tmp, sv, left;
  unsigned long long ex;
  asm volatile(
      "s_cmp_lg_u32 %[wv], 0\n\t"
      "s_cbranch_scc1 2f\n\t"
      "s_mov_b64 %[ex], exec\n\t"
      "s_mov_b64 exec, 1\n\t"
      "s_nop 1\n\t"
      ".if %c[add]\n\t"
      "global_atomic_add %[zero], %[one], %[cnt]\n\t"
      ".endif\n\t"
      "s_mov_b32 %[left], 0x100000\n\t"
      "1:\n\t"
      "global_load_dword %[tmp], %[zero], %[cnt] sc1\n\t"
      "s_waitcnt vmcnt(0)\n\t"
      "v_readfirstlane_b32 %[sv], %[tmp]\n\t"
      "s_cmp_ge_u32 %[sv], 8\n\t"
      "s_cbranch_scc1 3f\n\t"
      "s_sleep 1\n\t"
      "s_sub_u32 %[left], %[left], 1\n\t"
      "s_cmp_lg_u32 %[left], 0\n\t"
      "s_cbranch_scc1 1b\n\t"
      "3:\n\t"
      "s_mov_b64 exec, %[ex]\n\t"
      "2:\n\t"
      : [tmp] "=&v"(tmp), [sv] "=&s"(sv), [left] "=&s"(left), [ex] "=&s"(ex)
      : [wv] "s"(wv), [zero] "v"(zero), [one] "v"(one), [cnt] "s"(cnt), [add] "i"(ADD ? 1 : 0)
      : "memory", "scc");
}
DEV void raw_barrier() { asm volatile("" ::: "memory"); __builtin_amdgcn_s_barrier(); asm volatile("" ::: "memory"); }

template <int NT>
DEV void ffn_phase2(__amdgpu_buffer_rsrc_t rs, int wv, int lane, char* smem, int s, int mt, u16* out, bf16x8 (&ring)[RING2], char* ws) {
  const unsigned vo = lane * 16;
  constexpr int F0 = NT == 5 ? 36 : 0, NFR = 18 * NT, NA = NT - 2;
  auto ldw = [&](int idx) { return __builtin_bit_cast(bf16x8, __builtin_amdgcn_raw_buffer_load_b128(rs, vo, idx * 1024, 0)); };
  char* pbase = ws + WS_P + (size_t)mt * 8 * 73728;
  __amdgpu_buffer_rsrc_t rp = __builtin_amdgcn_make_buffer_rsrc(pbase, 0, 8 * 73728, 0x00020000);
  const char* cnt = ws + WS_CNT + mt * 128;
  bf16x8 b[2][4];
#pragma unroll
  for (int tt = 0; tt < 4; ++tt) b[0][tt] = *(const bf16x8*)(smem + K3_H + tt * 1024 + lane * 16);
#pragma unroll
  for (int gq = 0; gq < 2; ++gq) {
    const int NG = gq ? 2 : NA, I0 = gq ? 18 * NA : 0;
    f32x4 oc[3][4];
#pragma unroll
    for (int ks = 0; ks < 18; ++ks) {
      if (gq == 0 && ks == 17) asm volatile("" ::: "memory");
      if (gq == 0 || ks < 17) {
#pragma unroll
        for (int tt = 0; tt < 4; ++tt) b[(ks + 1) & 1][tt] = *(const bf16x8*)(smem + K3_H + ((ks + 1) % 18) * 4096 + tt * 1024 + lane * 16);
      }
      if (gq == 1 && ks == 8) {
        asm volatile("s_waitcnt vmcnt(0)" ::: "memory");
        raw_barrier();
        __builtin_amdgcn_raw_ptr_buffer_atomic_add_i32(1, __builtin_amdgcn_make_buffer_rsrc((void*)cnt, 0, 4, 0x00020000), (wv * 64 + lane) * 4, 0, 0);
      }
#pragma unroll
      for (int ti = 0; ti < NG; ++ti) {
        const int i = I0 + ks * NG + ti;
#pragma unroll
        for (int tt = 0; tt < 4; ++tt)
          oc[ti][tt] = __builtin_amdgcn_mfma_f32_16x16x32_bf16(ring[i % RING2], b[ks & 1][tt], ks == 0 ? f32x4{0.f, 0.f, 0.f, 0.f} : oc[ti][tt], 0, 0, 0);
        if (i + RING2 < NFR) ring[i % RING2] = ldw(F0 + i + RING2);
        __builtin_amdgcn_sched_barrier(0);
      }
    }
    const int slot0 = gq ? 40 + 4 * wv : (NT == 5 ? 6 * wv : 24 + 4 * (wv - 4));
#pragma unroll
    for (int ti = 0; ti < NG; ++ti)
#pragma unroll
      for (int tp = 0; tp < 2; ++tp) {
        const f32x4 u = oc[ti][2 * tp], v = oc[ti][2 * tp + 1];
        __builtin_amdgcn_raw_buffer_store_b128(u32x4{pk2(u[0], u[1]), pk2(u[2], u[3]), pk2(v[0], v[1]), pk2(v[2], v[3])}, rp,
                                               vo + (s * 72 + slot0) * 1024 + (ti * 2 + tp) * 1024, 0, 16  );
      }
    __builtin_amdgcn_sched_barrier(0);
  }
  const bool hasA = wv >= 1 && wv <= 5, hasB = wv >= 6 || wv <= 1;
  int slot[2], d0[2], t0[2];
  {
    slot[0] = 5 * s + wv - 1;
    const int ai = slot[0] >> 1, aq = (ai * 11) >> 5;
    const int dta = ai < 12 ? 5 * aq + (ai - 3 * aq) : 20 + 4 * ((ai - 12) >> 1) + (ai & 1);
    const int bs = 4 * s + ((wv + 2) & 3), bi = bs >> 1, bw = bi >> 1;
    slot[1] = 40 + bs;
    const int dtb = (bw < 4 ? 5 * bw + 3 : 4 * bw + 6) + (bi & 1);
    d0[0] = 16 * dta + 4 * (lane >> 4); d0[1] = 16 * dtb + 4 * (lane >> 4);
    t0[0] = 64 * mt + 32 * (slot[0] & 1) + (lane & 15); t0[1] = 64 * mt + 32 * (bs & 1) + (lane & 15);
  }
  const u16* x1T = (const u16*)(ws + WS_X1T) + mt * (D * 64 - 64);
  const u16* mf = (const u16*)(ws + WS_MODF);
  unsigned xr[2][8]; u32x2 gb[2];
#pragma unroll
  for (int k = 0; k < 2; ++k)
    if (k ? hasB : hasA) {
#pragma unroll
      for (int e = 0; e < 4; ++e) { xr[k][e] = x1T[(d0[k] + e) * 64 + t0[k]]; xr[k][4 + e] = x1T[(d0[k] + e) * 64 + t0[k] + 16]; }
      gb[k] = *(const u32x2*)(mf + 2 * D + d0[k]);
    }
  asm volatile("s_waitcnt vmcnt(0)" ::: "memory");
  arrive_wait<false>(cnt, wv);
  raw_barrier();
  u32x4 pv[2][8];
  if (hasA)
#pragma unroll
    for (int q = 0; q < 8; ++q) pv[0][q] = __builtin_amdgcn_raw_buffer_load_b128(rp, vo, q * 73728 + slot[0] * 1024, 16  );
  arrive_wait<true>(cnt + 4096, wv);
  raw_barrier();
  if (hasB)
#pragma unroll
    for (int q = 0; q < 8; ++q) pv[1][q] = __builtin_amdgcn_raw_buffer_load_b128(rp, vo, q * 73728 + slot[1] * 1024, 16  );
#pragma unroll
  for (int k = 0; k < 2; ++k)
    if (k ? hasB : hasA) {
      float o[8];
#pragma unroll
      for (int e = 0; e < 8; ++e) {
        const float a = ((elem(pv[k][0], e) + elem(pv[k][1], e)) + (elem(pv[k][2], e) + elem(pv[k][3], e))) +
                        ((elem(pv[k][4], e) + elem(pv[k][5], e)) + (elem(pv[k][6], e) + elem(pv[k][7], e)));
        const unsigned gw = gb[k][(e & 3) >> 1];
        o[e] = bf2f(xr[k][e]) + rb(((e & 1) ? bfhi(gw) : bflo(gw)) * rb(a));
      }
      *(u32x2*)(out + (size_t)t0[k] * D + d0[k]) = u32x2{pk2(o[0], o[1]), pk2(o[2], o[3])};
      *(u32x2*)(out + (size_t)(t0[k] + 16) * D + d0[k]) = u32x2{pk2(o[4], o[5]), pk2(o[6], o[7])};
    }
}

DEV void ffn_phase1(const char* wp, const char* whead, __amdgpu_buffer_rsrc_t rs, int wv, int lane, char* smem, u32x4 (&raw)[2][8], int s, int mt, u16* out, char* ws) {
  bf16x8 ring[RING];
  const unsigned vo = lane * 16;
  auto ldw = [&](int idx) { return __builtin_bit_cast(bf16x8, __builtin_amdgcn_raw_buffer_load_b128(rs, vo, idx * 1024, 0)); };
  unsigned boff[4];
#pragma unroll
  for (int tt = 0; tt < 4; ++tt) boff[tt] = tt * 1024 + ((lane ^ ((2 * tt + ((lane >> 3) & 1)) & 7)) * 16);
  f32x4 acc[8][4] = {};
  bf16x8 b[4];
#pragma unroll
  for (int tt = 0; tt < 4; ++tt) b[tt] = *(const bf16x8*)(smem + boff[tt]);
  bf16x8 a = tfrag(raw[0], 0);
  auto ldrow = [&](int j) {
    const char* p;
    asm("v_lshl_add_u64 %0, %1, 0, %2" : "=v"(p) : "v"(wp), "s"((unsigned long long)j * (F * 2)));
    return *(const __attribute__((address_space(1))) u32x4*)p;
  };
#define SGB(m, n) __builtin_amdgcn_sched_group_barrier(m, n, 0)
  auto step = [&](int par, int ksn, unsigned inc) {
    bf16x8 hi[4];
#pragma unroll
    for (int i = 0; i < 4; ++i) {
      bf16x8 an;
      if (i < 3) an = tfrag(raw[par], i + 1);
      hi[i] = tfrag(raw[par], 4 + i);
#pragma unroll
      for (int tt = 0; tt < 4; ++tt) acc[i][tt] = __builtin_amdgcn_mfma_f32_16x16x32_bf16(a, b[tt], acc[i][tt], 0, 0, 0);
      if (i < 3) a = an;
      SGB(0x8, 1); SGB(0x2, 2); SGB(0x8, 1); SGB(0x2, 2); SGB(0x8, 1); SGB(0x2, 2); SGB(0x8, 1); SGB(0x2, 2);
      __builtin_amdgcn_sched_barrier(0);
    }
#pragma unroll
    for (int i = 4; i < 6; ++i) {
#pragma unroll
      for (int j = 0; j < 3; ++j) raw[par][3 * (i - 4) + j] = ldrow(3 * (i - 4) + j);
#pragma unroll
      for (int tt = 0; tt < 4; ++tt) acc[i][tt] = __builtin_amdgcn_mfma_f32_16x16x32_bf16(hi[i - 4], b[tt], acc[i][tt], 0, 0, 0);
      SGB(0x8, 1); SGB(0x2, 1); SGB(0x20, 1); SGB(0x8, 1); SGB(0x2, 1); SGB(0x20, 1); SGB(0x8, 1); SGB(0x2, 1); SGB(0x20, 1); SGB(0x8, 1);
      __builtin_amdgcn_sched_barrier(0);
    }
    const char* bn = smem + ksn * 4096;
#pragma unroll
    for (int tt = 0; tt < 4; ++tt) {
      if (tt < 2) raw[par][6 + tt] = ldrow(6 + tt);
      if (tt == 2) { a = tfrag(raw[par ^ 1], 0); wp += inc; }
      acc[6][tt] = __builtin_amdgcn_mfma_f32_16x16x32_bf16(hi[2], b[tt], acc[6][tt], 0, 0, 0);
      acc[7][tt] = __builtin_amdgcn_mfma_f32_16x16x32_bf16(hi[3], b[tt], acc[7][tt], 0, 0, 0);
      b[tt] = *(const bf16x8*)(bn + boff[tt]);
      SGB(0x8, 1); SGB(0x2, 3); SGB(0x20, 1); SGB(0x8, 1); SGB(0x2, 3); SGB(0x100, 1);
      __builtin_amdgcn_sched_barrier(0);
    }
  };
#pragma unroll 1
  for (int it = 0; it < 9; ++it) {
    const bool last = it == 8;
    if (last) wp = whead;
    const unsigned inc = (last ? 8 : 32) * (F * 2);
    step(0, 2 * it + 1, inc);
    step(1, last ? 0 : 2 * it + 2, inc);
  }
#pragma unroll
  for (int i = 0; i < 16; ++i) ring[i] = __builtin_bit_cast(bf16x8, raw[i >> 3][i & 7]);
  char* hw0 = smem + K3_H + (2 * wv) * 4096 + lane * 16;
  auto put4 = [&](int h, int tt) {
    *(u32x4*)(hw0 + h * 4096 + tt * 1024) = u32x4{swiglu_tile(acc[4 * h][tt]), swiglu_tile(acc[4 * h + 1][tt]), swiglu_tile(acc[4 * h + 2][tt]), swiglu_tile(acc[4 * h + 3][tt])};
  };
  if (wv < 4) {
    put4(0, 0); put4(0, 1);
    __builtin_amdgcn_sched_barrier(0);
    f32x4 am[2][4] = {};
#pragma unroll
    for (int ks = 0; ks < 18; ++ks) {
#pragma unroll
      for (int tt = 0; tt < 4; ++tt) {
        am[0][tt] = __builtin_amdgcn_mfma_f32_16x16x32_bf16(ring[(2 * ks) % RING], b[tt], am[0][tt], 0, 0, 0);
        am[1][tt] = __builtin_amdgcn_mfma_f32_16x16x32_bf16(ring[(2 * ks + 1) % RING], b[tt], am[1][tt], 0, 0, 0);
        if (ks < 17) b[tt] = *(const bf16x8*)(smem + (ks + 1) * 4096 + boff[tt]);
      }
      ring[(2 * ks) % RING] = ldw(2 * ks + RING);
      ring[(2 * ks + 1) % RING] = ldw(2 * ks + 1 + RING);
      if (ks < 18 && ks % 3 == 0) put4((ks / 3 + 2) >> 2, (ks / 3 + 2) & 3);
      __builtin_amdgcn_sched_barrier(0);
    }
    char* hw8 = smem + K3_H + (16 + (wv >> 1)) * 4096 + lane * 16 + (wv & 1) * 8;
#pragma unroll
    for (int tt = 0; tt < 4; ++tt) *(u32x2*)(hw8 + tt * 1024) = u32x2{swiglu_tile(am[0][tt]), swiglu_tile(am[1][tt])};
    bf16x8 rg[RING2];
#pragma unroll
    for (int k = 0; k < RING2; ++k) rg[k] = k < RING ? ring[(36 + k) % RING] : ldw(36 + k);
    lds_barrier();
    ffn_phase2<5>(rs, wv, lane, smem, s, mt, out, rg, ws);
    __builtin_amdgcn_endpgm();
  }
  bf16x8 rg[RING2];
#pragma unroll
  for (int k = 0; k < RING; ++k) rg[k] = ring[k];
#pragma unroll
  for (int q = 0; q < 8; ++q) {
    put4(q >> 2, q & 3);
#pragma unroll
    for (int k = RING + 4 * q; k < RING + 4 * q + 4; ++k)
      if (k < RING2) rg[k] = ldw(k);
    __builtin_amdgcn_sched_barrier(0);
  }
  lds_barrier();
  ffn_phase2<4>(rs, wv, lane, smem, s, mt, out, rg, ws);
  __builtin_amdgcn_endpgm();
}
__global__ __launch_bounds__(512, 2) void k3_ffn(const u16* w1, const u16* w3, char* ws, u16* out) {
  extern __shared__ __attribute__((aligned(16))) char smem[];
  const int tid = threadIdx.x, lane = tid & 63;
  const int wv = __builtin_amdgcn_readfirstlane(tid >> 6);
  const int xc = blockIdx.x & 7, bi = blockIdx.x >> 3;
  const int s = (xc % (8 / SPX)) * SPX + bi % SPX, mt = (xc / (8 / SPX)) * (32 / SPX) + bi / SPX;
  const u16* x1T = (const u16*)(ws + WS_X1T) + mt * (D * 64);
  const u16* mf = (const u16*)(ws + WS_MODF);
  const int tc = tid & 7, cg = tid >> 3;
  u32x4 r[9];
#pragma unroll
  for (int e = 0; e < 8; ++e) r[e] = *(const u32x4*)(x1T + (8 * cg + e) * 64 + 8 * tc);
  r[8] = *(const u32x4*)(x1T + (512 + cg) * 64 + 8 * tc);
  const u32x4 sbv = *(const u32x4*)(mf + 8 * cg), cbv = *(const u32x4*)(mf + D + 8 * cg);
  const float sb8 = bf2f(mf[512 + cg]), cb8 = bf2f(mf[D + 512 + cg]);
  __builtin_amdgcn_sched_barrier(0);
  __builtin_amdgcn_sched_barrier(0);
  f32x2 xv[9][4];
#pragma unroll
  for (int e = 0; e < 9; ++e)
#pragma unroll
    for (int t = 0; t < 4; ++t) xv[e][t] = f32x2{bflo(r[e][t]), bfhi(r[e][t])};
  float s1, s2;
  {
    f32x2 p1[4], p2[4];
#pragma unroll
    for (int t = 0; t < 4; ++t) {
      p1[t] = xv[0][t]; p2[t] = xv[0][t] * xv[0][t];
#pragma unroll
      for (int e = 1; e < 9; ++e) { p1[t] += xv[e][t]; p2[t] += xv[e][t] * xv[e][t]; }
    }
    auto sw32 = [](float a, float b) { auto q = __builtin_amdgcn_permlane32_swap(__float_as_uint(a), __float_as_uint(b), false, false); return __uint_as_float(q[0]) + __uint_as_float(q[1]); };
    auto sw16 = [](float a, float b) { auto q = __builtin_amdgcn_permlane16_swap(__float_as_uint(a), __float_as_uint(b), false, false); return __uint_as_float(q[0]) + __uint_as_float(q[1]); };
    float u1[4], u2[4];
#pragma unroll
    for (int t = 0; t < 4; ++t) { u1[t] = sw32(p1[t >> 1][t & 1], p1[2 + (t >> 1)][t & 1]); u2[t] = sw32(p2[t >> 1][t & 1], p2[2 + (t >> 1)][t & 1]); }
    const float v10 = sw16(u1[0], u1[2]), v11 = sw16(u1[1], u1[3]), v20 = sw16(u2[0], u2[2]), v21 = sw16(u2[1], u2[3]);
    const bool b3 = lane & 8;
    auto ror8 = [](float v) { return __int_as_float(__builtin_amdgcn_update_dpp(0, __float_as_int(v), 0x128, 0xf, 0xf, false)); };
    s1 = (b3 ? v11 : v10) + ror8(b3 ? v10 : v11);
    s2 = (b3 ? v21 : v20) + ror8(b3 ? v20 : v21);
  }
  float* st = (float*)(smem + K3_ST);
  *(f32x2*)(st + (wv * 64 + 8 * tc + (lane >> 3)) * 2) = f32x2{s1, s2};
  __builtin_amdgcn_sched_barrier(0);
  char* wbase = ws + WS_WT + ((size_t)s * FR_S + wave_frag_off(wv)) * 1024;
  __amdgpu_buffer_rsrc_t rs = __builtin_amdgcn_make_buffer_rsrc(wbase, 0, 126 * 1024, 0x00020000);
  const char* wp = (const char*)((lane & 8) ? w3 : w1) + ((size_t)(8 * (lane >> 4)) * F + 576 * s + 64 * wv + 8 * (lane & 7)) * 2;
  u32x4 raw[2][8];
#pragma unroll
  for (int i = 0; i < 2; ++i) {
#pragma unroll
    for (int j = 0; j < 8; ++j) raw[i][j] = *(const u32x4*)(wp + (size_t)j * (F * 2));
    wp += (size_t)32 * (F * 2);
  }
  __builtin_amdgcn_sched_barrier(0);
  lds_barrier();
  float* sw = (float*)(smem + K3_ST + 4096) + wv * 128;
  {
    float a = 0.f, b = 0.f;
#pragma unroll
    for (int w = 0; w < 8; ++w) { f32x2 v = *(const f32x2*)(st + (w * 64 + lane) * 2); a += v.x; b += v.y; }
    const float mean = a * (1.f / D);
    const float var = fmaxf(b * (1.f / D) - mean * mean, 0.f);
    const float rd = __builtin_amdgcn_rcpf(rb(__builtin_amdgcn_sqrtf(rb(rb(var) + 1e-5f))));
    sw[lane] = rd; sw[64 + lane] = -rb(mean) * rd;
  }
  const f32x4 rd0 = *(const f32x4*)(sw + 8 * tc), rd1 = *(const f32x4*)(sw + 8 * tc + 4);
  const f32x4 nm0 = *(const f32x4*)(sw + 64 + 8 * tc), nm1 = *(const f32x4*)(sw + 64 + 8 * tc + 4);
  const f32x2 rdp[4] = {{rd0[0], rd0[1]}, {rd0[2], rd0[3]}, {rd1[0], rd1[1]}, {rd1[2], rd1[3]}};
  const f32x2 nmp[4] = {{nm0[0], nm0[1]}, {nm0[2], nm0[3]}, {nm1[0], nm1[1]}, {nm1[2], nm1[3]}};
  char* base = smem + (cg >> 2) * 4096 + (tc >> 1) * 1024 + (cg & 3) * 256 + (tc & 1) * 128;
  char* base8 = smem + (16 + (wv >> 2)) * 4096 + (tc >> 1) * 1024 + (wv & 3) * 256 + (tc & 1) * 128 + (cg & 7) * 2;
#pragma unroll
  for (int t = 0; t < 4; ++t) {
    f32x2 hv[9];
#pragma unroll
    for (int e = 0; e < 9; ++e) {
      const float cb = e < 8 ? elem(cbv, e) : cb8, sb = e < 8 ? elem(sbv, e) : sb8;
      hv[e] = __builtin_elementwise_fma(__builtin_elementwise_fma(xv[e][t], rdp[t], nmp[t]), f32x2{cb, cb}, f32x2{sb, sb});
    }
#pragma unroll
    for (int p = 0; p < 2; ++p) {
      *(u32x4*)(base + (((2 * t + p) ^ tc) * 16)) = u32x4{pk2(hv[0][p], hv[1][p]), pk2(hv[2][p], hv[3][p]), pk2(hv[4][p], hv[5][p]), pk2(hv[6][p], hv[7][p])};
      *(u16*)(base8 + (((2 * t + p) ^ tc) * 16)) = (u16)pk2(hv[8][p], 0.f);
    }
  }
  lds_barrier();
  ffn_phase1(wp, ws + WS_WT + ((size_t)s * FR_S + wv) * 1024 + lane * 16, rs, wv, lane, smem, raw, s, mt, out, ws);
}

extern "C" void kernel_launch(void* const* d_in, const int* in_sizes, int n_in, void* d_out, int out_size, void* d_ws,
                              size_t ws_size, hipStream_t stream) {
  const u16* x = (const u16*)d_in[0];
  const u16 *Are = (const u16*)d_in[1], *Aim = (const u16*)d_in[2], *Bre = (const u16*)d_in[3], *Bim = (const u16*)d_in[4];
  const u16 *Cre = (const u16*)d_in[5], *Cim = (const u16*)d_in[6], *logdt = (const u16*)d_in[7], *Dskip = (const u16*)d_in[8];
  const u16 *w1 = (const u16*)d_in[10], *w3 = (const u16*)d_in[11], *w2 = (const u16*)d_in[12];
  const u16 *c = (const u16*)d_in[13], *wmod = (const u16*)d_in[14];
  char* ws = (char*)d_ws;
  (void)hipFuncSetAttribute((const void*)k3_ffn, hipFuncAttributeMaxDynamicSharedMemorySize, K3_LDS);
  k0_prep<<<920 + RT0, 256, K0_LDS, stream>>>(x, Are, Aim, Bre, Bim, Cre, Cim, logdt, w1, w3, w2, c, wmod, ws);
  k1_conv<<<D, 256, K1_LDS, stream>>>(Dskip, w1, w3, w2, ws);
  k3_ffn<<<256, 512, K3_LDS, stream>>>(w1, w3, ws, (u16*)d_out);
}
```
